```python
import torch

def _softmax(x, axis=-1):
    return torch.softmax(x, dim=axis)
def _gelu(x):
    return torch.nn.functional.gelu(x, approximate="tanh")
def _silu(x):
    return torch.nn.functional.silu(x)
def _sigmoid(x):
    return torch.sigmoid(x)
def _layernorm(x, axis=-1, eps=1e-5):
    m = x.mean(dim=axis, keepdim=True)
    v = x.var(dim=axis, keepdim=True, unbiased=False)
    return (x - m) / torch.sqrt(v + eps)
def _scatter_add(tgt, idx, src, axis=0):
    return tgt.clone().index_add_(axis, idx, src)
def _segment_sum(src, seg, n):
    out = torch.zeros((n,) + src.shape[1:], dtype=src.dtype)
    return out.index_add_(0, seg, src)
def _pad(x, pw):
    flat = tuple(v for pair in reversed(pw) for v in pair)
    return torch.nn.functional.pad(x, flat)

kernel_name = 'parallel_sum__mha_block_cached__dense_activation_gelu__decode'

def setup_inputs():

    _w = (lambda a: (lambda t: t.to(torch.bfloat16) if t.is_floating_point() else t)(torch.as_tensor(a)))
    def _A_setup():
        B, Sq, D = 8, 1, 512
        H, Sk, Dh = 32, 4096, 128
        return {
            "x": _w((lambda *s: torch.randn(*s, dtype=torch.bfloat16) * 0.05)(B, Sq, D)),
            "k_cache": _w((lambda *s: torch.randn(*s, dtype=torch.bfloat16) * 3.2)(B, H, Sk, Dh)),
            "v_cache": _w((lambda *s: torch.randn(*s, dtype=torch.bfloat16) * 0.8)(B, H, Sk, Dh)),
            "wq": _w((lambda *s: torch.randn(*s, dtype=torch.bfloat16) * 3.2)(D, H * Dh) * (D ** -0.5)),
            "wo": _w((lambda *s: torch.randn(*s, dtype=torch.bfloat16) * 0.8)(H * Dh, D) * ((H * Dh) ** -0.5)),
        }
    _A_in = _A_setup()

    def _B_setup():
        B, S, D, F = 8, 1, 512, 1792
        return {
            "x": _w((lambda *s: torch.randn(*s, dtype=torch.bfloat16) * 0.05)(B, S, D)),
            "w1": _w((lambda *s: torch.randn(*s, dtype=torch.bfloat16) * 3.2)(D, F) * (D ** -0.5)),
            "b1": _w((lambda *s: torch.randn(*s, dtype=torch.bfloat16) * 3.2)(F) * 0.01),
            "w2": _w((lambda *s: torch.randn(*s, dtype=torch.bfloat16) * 0.4)(F, D) * (F ** -0.5)),
            "b2": _w((lambda *s: torch.randn(*s, dtype=torch.bfloat16) * 0.4)(D) * 0.01),
        }
    _B_in = _B_setup()

    return {
        "A_x": _A_in["x"],
        "A_k_cache": _A_in["k_cache"],
        "A_v_cache": _A_in["v_cache"],
        "A_wq": _A_in["wq"],
        "A_wo": _A_in["wo"],
        "B_x": _B_in["x"],
        "B_w1": _B_in["w1"],
        "B_b1": _B_in["b1"],
        "B_w2": _B_in["w2"],
        "B_b2": _B_in["b2"],
    }

def reference(A_x, A_k_cache, A_v_cache, A_wq, A_wo, B_x, B_w1, B_b1, B_w2, B_b2):
    def _A_ref(x, k_cache, v_cache, wq, wo):
        B, Sq, D = x.shape
        H, Dh = 32, 128
        q = (lambda x, *d: x.permute(*d))((x @ wq).reshape(B, Sq, H, Dh), 0, 2, 1, 3)
        scores = torch.einsum("bhqd,bhkd->bhqk", q, k_cache) * (Dh ** -0.5)
        p = _softmax(scores, axis=-1)
        o = torch.einsum("bhqk,bhkd->bhqd", p, v_cache)
        return (lambda x, *d: x.permute(*d))(o, 0, 2, 1, 3).reshape(B, Sq, H * Dh) @ wo

    def _B_ref(x, w1, b1, w2, b2):
        h = torch.einsum("bsd,df->bsf", x, w1) + b1
        h = _gelu(h)
        return torch.einsum("bsf,fd->bsd", h, w2) + b2

    x = A_x
    xn = _layernorm(x, axis=-1)
    return x + _A_ref(xn, A_k_cache, A_v_cache, A_wq, A_wo) + _B_ref(xn, B_w1, B_b1, B_w2, B_b2)
```

```cpp
#include <hip/hip_runtime.h>
#include <hip/hip_bf16.h>
#include <stdint.h>

#define FUSED_TAIL 1
#define PRE 10
#define WIN 6
#define HPACE 2
#define POLL_THR 28
#define POLL_SLEEP 8
#define PRIO 1

typedef unsigned u32x4 __attribute__((ext_vector_type(4)));
typedef unsigned u32x2 __attribute__((ext_vector_type(2)));
typedef float f32x2 __attribute__((ext_vector_type(2)));
typedef float f32x4 __attribute__((ext_vector_type(4)));
typedef __bf16 bf16x2 __attribute__((ext_vector_type(2)));
typedef unsigned short u16;
#define GAS __attribute__((address_space(1)))

constexpr int NB = 8, NH = 32, SK = 4096, DH = 128, DM = 512, FF = 1792, FS = FF / NH;
constexpr int NW = 8, NTHR = NW * 64;
constexpr int NS = 4;
constexpr int NU = SK / (16 * NS);
constexpr float SCALE = 0.088388347648318440f, LOG2E = 1.4426950408889634f;

constexpr size_t WS_EPOCH = 0, WS_GRAN = 4096;
#define STAMP(k)
#define STAMPW(w, k)

__device__ __forceinline__ float dot2(unsigned a, unsigned b, float acc) {
  return __builtin_amdgcn_fdot2_f32_bf16(__builtin_bit_cast(bf16x2, a), __builtin_bit_cast(bf16x2, b), acc, false);
}
template <int CTRL> __device__ __forceinline__ float dpp(float x) {
  return __builtin_bit_cast(float, __builtin_amdgcn_mov_dpp(__builtin_bit_cast(int, x), CTRL, 0xf, 0xf, true));
}
constexpr int ROR1 = 0x121, ROR2 = 0x122, ROR4 = 0x124, ROR8 = 0x128, XOR1 = 0xB1, XOR2 = 0x4E;
__device__ __forceinline__ float row_sum16(float x) {
  x += dpp<ROR8>(x); x += dpp<ROR4>(x); x += dpp<ROR2>(x); x += dpp<ROR1>(x); return x;
}
__device__ __forceinline__ float row_max16(float x) {
  x = fmaxf(x, dpp<ROR8>(x)); x = fmaxf(x, dpp<ROR4>(x)); x = fmaxf(x, dpp<ROR2>(x)); x = fmaxf(x, dpp<ROR1>(x)); return x;
}
__device__ __forceinline__ float xrow_sum(float x) {
  auto s = __builtin_amdgcn_permlane16_swap(__float_as_uint(x), __float_as_uint(x), false, false);
  x = __uint_as_float(s[0]) + __uint_as_float(s[1]);
  auto t = __builtin_amdgcn_permlane32_swap(__float_as_uint(x), __float_as_uint(x), false, false);
  return __uint_as_float(t[0]) + __uint_as_float(t[1]);
}
__device__ __forceinline__ float xrow_max(float x) {
  auto s = __builtin_amdgcn_permlane16_swap(__float_as_uint(x), __float_as_uint(x), false, false);
  x = fmaxf(__uint_as_float(s[0]), __uint_as_float(s[1]));
  auto t = __builtin_amdgcn_permlane32_swap(__float_as_uint(x), __float_as_uint(x), false, false);
  return fmaxf(__uint_as_float(t[0]), __uint_as_float(t[1]));
}
__device__ __forceinline__ void xrow_reduce8(const float (&v)[8], float& z0, float& z1) {
  float g[4];
#pragma unroll
  for (int k = 0; k < 4; ++k) {
    auto s = __builtin_amdgcn_permlane16_swap(__float_as_uint(v[2 * k]), __float_as_uint(v[2 * k + 1]), false, false);
    g[k] = __uint_as_float(s[0]) + __uint_as_float(s[1]);
  }
  auto a = __builtin_amdgcn_permlane32_swap(__float_as_uint(g[0]), __float_as_uint(g[1]), false, false);
  z0 = __uint_as_float(a[0]) + __uint_as_float(a[1]);
  auto b = __builtin_amdgcn_permlane32_swap(__float_as_uint(g[2]), __float_as_uint(g[3]), false, false);
  z1 = __uint_as_float(b[0]) + __uint_as_float(b[1]);
}
__device__ __forceinline__ float bf_lo(unsigned u) { return __uint_as_float(u << 16); }
__device__ __forceinline__ float bf_hi(unsigned u) { return __uint_as_float(u & 0xffff0000u); }
__device__ __forceinline__ unsigned pk_bf16(float lo, float hi) {
  bf16x2 r = {(__bf16)lo, (__bf16)hi};
  return __builtin_bit_cast(unsigned, r);
}
__device__ __forceinline__ float rnd_bf16(float x) { return (float)(__bf16)x; }
__device__ __forceinline__ void gemv_pair(const u32x4& a, const u32x4& b, unsigned w, float (&acc)[8]) {
#pragma unroll
  for (int j = 0; j < 4; ++j) {
    acc[2 * j]     = dot2(__builtin_amdgcn_perm(b[j], a[j], 0x05040100u), w, acc[2 * j]);
    acc[2 * j + 1] = dot2(__builtin_amdgcn_perm(b[j], a[j], 0x07060302u), w, acc[2 * j + 1]);
  }
}
#define FENCE() do { asm volatile("" ::: "memory"); __builtin_amdgcn_sched_barrier(0); } while (0)
#define NOW 0
#define WIN0 WIN
#define ENDB 0
__device__ __forceinline__ u32x4 ldq(const u16* p) {
  return __builtin_nontemporal_load(reinterpret_cast<const u32x4*>(p));
}
__device__ __forceinline__ u32x4 ldw(const u16* p) {
  return __builtin_nontemporal_load(reinterpret_cast<const u32x4*>(p));
}
template <int N> __device__ __forceinline__ void vmwait() { asm volatile("s_waitcnt vmcnt(%0)" :: "i"(N) : "memory"); __builtin_amdgcn_sched_barrier(0); }

constexpr int L_S = 0, L_Q = L_S + SK, L_H = L_Q + 4 * 128, L_REC = L_H + 4 * 64, L_ML = L_REC + 4 * 128, L_XN = L_ML + 8,
              L_CNT = L_XN + 4 * 256, L_END = L_CNT + 4;
#define LDS_ARRIVE(i) do { asm volatile("s_waitcnt lgkmcnt(0)" ::: "memory"); \
    if (lane == 0) __hip_atomic_fetch_add(reinterpret_cast<unsigned*>(lds) + L_CNT + (i), 1u, __ATOMIC_RELAXED, __HIP_MEMORY_SCOPE_WORKGROUP); } while (0)
#define LDS_WAIT(i, slp) do { \
    while (__hip_atomic_load(reinterpret_cast<unsigned*>(lds) + L_CNT + (i), __ATOMIC_RELAXED, __HIP_MEMORY_SCOPE_WORKGROUP) < 4u) \
      __builtin_amdgcn_s_sleep(slp); \
    asm volatile("" ::: "memory"); } while (0)
#define STR2(x) #x
#define STR(x) STR2(x)
#define VMWAIT(n) do { asm volatile("s_waitcnt vmcnt(" STR(n) ")" ::: "memory"); __builtin_amdgcn_sched_barrier(0); } while (0)
template <int K> __device__ __forceinline__ unsigned row_bcast(unsigned v) {
  return (unsigned)__builtin_amdgcn_mov_dpp((int)v, 0x150 + K, 0xf, 0xf, true);
}
template <int N, int I = 0> __device__ __forceinline__ void gemv_bcast(const unsigned (&wp)[N][8], unsigned act, int, float (&y)[8]) {
  if constexpr (I < N) {
    const unsigned a = row_bcast<I>(act);
#pragma unroll
    for (int j = 0; j < 8; ++j) y[j] = dot2(wp[I][j], a, y[j]);
    gemv_bcast<N, I + 1>(wp, act, 0, y);
  }
}
template <int N, int I = 0> __device__ __forceinline__ void gemv_bcast2(const u32x4 (&wv)[2 * N], unsigned act, float (&y)[8]) {
  if constexpr (I < N) {
    gemv_pair(wv[2 * I], wv[2 * I + 1], row_bcast<2 * I>(act), y);
    gemv_bcast2<N, I + 1>(wv, act, y);
  }
}

__global__ void __launch_bounds__(NTHR, 2)
fused_decode(const u16* __restrict__ x, const u16* __restrict__ Kg, const u16* __restrict__ Vg, const u16* __restrict__ wq,
             const u16* __restrict__ wo, const u16* __restrict__ w1, const u16* __restrict__ b1, const u16* __restrict__ w2,
             const u16* __restrict__ b2, u16* __restrict__ out, unsigned char* __restrict__ ws) {
  __shared__ __attribute__((aligned(16))) float lds[L_END];
  const int bid = blockIdx.x, h = (bid & 7) * 4 + ((bid >> 3) & 3), b = bid >> 5;
  const int tid = threadIdx.x, lane = tid & 63, wave = __builtin_amdgcn_readfirstlane(tid >> 6), r = lane >> 4, c = lane & 15;
  STAMPW(0, 0);
  const u16* xb = x + b * DM;
  GAS unsigned* epoch = (GAS unsigned*)(ws + WS_EPOCH) + 64 * b;
  GAS unsigned long long* gran = (GAS unsigned long long*)(ws + WS_GRAN);

  if (wave < NS) {
    __builtin_amdgcn_s_setprio(1);
    const size_t kvbase = (size_t)(b * NH + h) * SK * DH;
    auto rk = __builtin_amdgcn_make_buffer_rsrc((void*)(Kg + kvbase), 0, SK * DH * 2, 0x00020000);
    auto rv = __builtin_amdgcn_make_buffer_rsrc((void*)(Vg + kvbase), 0, SK * DH * 2, 0x00020000);
    const int rot = (bid >> 3) * 2;
    const unsigned voff = lane * 16 + wave * 4096;
#define LD(rs, u, t) __builtin_amdgcn_raw_buffer_load_b128(rs, voff + 1024 * (t), (((u) + rot) & (NU - 1)) * (NS * 4096), 2)
#define WINWAIT() VMWAIT(5)
    u32x4 R[PRE][4];
#pragma unroll
    for (int n = 0; n < WIN0; ++n) R[n >> 2][n & 3] = LD(rk, n >> 2, n & 3);
    FENCE();
    if (tid < 4) reinterpret_cast<unsigned*>(lds)[L_CNT + tid] = 0u;
    __syncthreads();
#pragma unroll
    for (int n = WIN0; n < 4 * PRE; ++n) { if (n < 16) vmwait<WIN0 - 1>(); else WINWAIT(); R[n >> 2][n & 3] = LD(rk, n >> 2, n & 3); FENCE(); }
    STAMPW(0, 1);
    LDS_WAIT(0, 1);
    STAMPW(0, 2);
    u32x4 qp;
    {
      f32x4 a = {0, 0, 0, 0}, bq = {0, 0, 0, 0};
#pragma unroll
      for (int w = 0; w < 4; ++w) {
        a += *reinterpret_cast<const f32x4*>(&lds[L_Q + w * 128 + 8 * c]);
        bq += *reinterpret_cast<const f32x4*>(&lds[L_Q + w * 128 + 8 * c + 4]);
      }
      qp = u32x4{pk_bf16(a[0], a[1]), pk_bf16(a[2], a[3]), pk_bf16(bq[0], bq[1]), pk_bf16(bq[2], bq[3])};
    }
    float* sw = &lds[L_S + wave * 1024 + r * 4 + (c & 3)];
    const bool b0 = (c & 1) != 0, b1s = (c & 2) != 0;
#define KSTEP(s_, u_, ISSUE_, rs_, nu_) do { \
      float sc[4]; \
      _Pragma("unroll") for (int t = 0; t < 4; ++t) { \
        sc[t] = dot2(R[s_][t][3], qp[3], dot2(R[s_][t][2], qp[2], dot2(R[s_][t][1], qp[1], dot2(R[s_][t][0], qp[0], 0.f)))); \
        asm volatile("" : "+v"(sc[t]) :: "memory"); \
        __builtin_amdgcn_sched_barrier(0); \
        if (ISSUE_) { if (WIN < 8) WINWAIT(); R[s_][t] = LD(rs_, nu_, t); } \
        __builtin_amdgcn_sched_barrier(0); \
      } \
        \
      const float e0 = (b0 ? sc[1] : sc[0]) + dpp<XOR1>(b0 ? sc[0] : sc[1]); \
      const float e1 = (b0 ? sc[3] : sc[2]) + dpp<XOR1>(b0 ? sc[2] : sc[3]); \
      float f = (b1s ? e1 : e0) + dpp<XOR2>(b1s ? e0 : e1); \
      f += dpp<ROR4>(f); f += dpp<ROR8>(f); \
      if (c < 4) sw[(u_) * 16] = f; \
      __builtin_amdgcn_sched_barrier(0); \
    } while (0)
    KSTEP(0, 0, false, rk, 0);
    KSTEP(1, 1, false, rk, 0);
#pragma unroll
    for (int k = 0; k < 8; ++k) {
      WINWAIT(); R[k >> 2][k & 3] = LD(rk, PRE + (k >> 2), k & 3); FENCE();
      if (2 + k < PRE) KSTEP(2 + k, 2 + k, false, rk, 0);
    }
#pragma unroll
    for (int k = 10; k < PRE; ++k) KSTEP(k, k, false, rk, 0);
    static_assert(PRE % 2 == 0 && PRE >= 4, "ring parity");
    for (int u = PRE; u < NU - 2; u += 2) { KSTEP(0, u, true, rk, u + 2); KSTEP(1, u + 1, true, rk, u + 3); }
    KSTEP(0, NU - 2, true, rv, 0); KSTEP(1, NU - 1, true, rv, 1);
    FENCE();
    STAMPW(0, 12); STAMPW(3, 13);
    {
      asm volatile("s_waitcnt lgkmcnt(0)" ::: "memory");
      float* sp = &lds[L_S + wave * 1024 + 4 * lane];
      f32x4 s[4];
#pragma unroll
      for (int k = 0; k < 4; ++k) s[k] = *reinterpret_cast<const f32x4*>(sp + 256 * k);
      FENCE();
      WINWAIT(); R[2][0] = LD(rv, 2, 0); FENCE();
      float m = -INFINITY;
#pragma unroll
      for (int k = 0; k < 4; ++k)
#pragma unroll
        for (int j = 0; j < 4; ++j) { s[k][j] = rnd_bf16(rnd_bf16(s[k][j]) * SCALE); m = fmaxf(m, s[k][j]); }
      asm volatile("" : "+v"(m));
      FENCE();
      WINWAIT(); R[2][1] = LD(rv, 2, 1); FENCE();
      m = xrow_max(row_max16(m));
      float l = 0.f;
#pragma unroll
      for (int k = 0; k < 4; ++k) {
#pragma unroll
        for (int j = 0; j < 4; ++j) { s[k][j] = __builtin_amdgcn_exp2f((s[k][j] - m) * LOG2E); l += s[k][j]; }
        *reinterpret_cast<f32x4*>(sp + 256 * k) = s[k];
        if (k == 1) { FENCE(); WINWAIT(); R[2][2] = LD(rv, 2, 2); FENCE(); }
      }
      FENCE();
      WINWAIT(); R[2][3] = LD(rv, 2, 3); FENCE();
      l = xrow_sum(row_sum16(l));
      if (lane == 0) *reinterpret_cast<f32x2*>(&lds[L_ML + 2 * wave]) = f32x2{m, l};
      LDS_ARRIVE(2);
    }
    f32x2 acc[4] = {{0, 0}, {0, 0}, {0, 0}, {0, 0}};
    const float* pr = &lds[L_S + wave * 1024 + r * 4];
    f32x4 pc = *reinterpret_cast<const f32x4*>(pr);
#define VSTEP(s_, u_, ISSUE_, PACE_, nu_) do { \
      const f32x4 pn = *reinterpret_cast<const f32x4*>(pr + (((u_) + 1) & (NU - 1)) * 16); \
      _Pragma("unroll") for (int t = 0; t < 4; ++t) { \
        _Pragma("unroll") for (int j = 0; j < 4; ++j) { \
          const f32x2 vv = {bf_lo(R[s_][t][j]), bf_hi(R[s_][t][j])}; \
          acc[j] = __builtin_elementwise_fma(vv, f32x2{pc[t], pc[t]}, acc[j]); } \
        asm volatile("" : "+v"(acc[0]), "+v"(acc[1]), "+v"(acc[2]), "+v"(acc[3]) :: "memory"); \
        __builtin_amdgcn_sched_barrier(0); \
        if (ISSUE_) { if (PACE_) WINWAIT(); R[s_][t] = LD(rv, nu_, t); } \
        __builtin_amdgcn_sched_barrier(0); \
      } \
      pc = pn; \
    } while (0)
    VSTEP(0, 0, true, true, 3); VSTEP(1, 1, false, false, 0);
    for (int u = 2; u < NU - 2 - ENDB; u += 2) { VSTEP(2, u, true, WIN < 8, u + 2); VSTEP(0, u + 1, true, WIN < 8, u + 3); }
#pragma unroll
    for (int k = 0; k < ENDB; ++k) {
#pragma unroll
      for (int t = 0; t < 4; ++t) R[k == 0 ? 1 : k + 2][t] = LD(rv, NU - ENDB + k, t);
    }
    FENCE();
    float fw;
    {
      LDS_WAIT(2, 1);
      const f32x2 ml = *reinterpret_cast<const f32x2*>(&lds[L_ML + 2 * (lane & 3)]);
      float M = fmaxf(ml[0], dpp<XOR1>(ml[0])); M = fmaxf(M, dpp<XOR2>(M));
      const float e = __builtin_amdgcn_exp2f((ml[0] - M) * LOG2E);
      float L = ml[1] * e; L += dpp<XOR1>(L); L += dpp<XOR2>(L);
      fw = __uint_as_float(__builtin_amdgcn_readlane(__float_as_uint(e / L), wave));
    }
    FENCE();
    VSTEP(2, NU - 2 - ENDB, false, false, 0); VSTEP(0, NU - 1 - ENDB, false, false, 0);
#pragma unroll
    for (int k = 0; k < ENDB; ++k) VSTEP(k == 0 ? 1 : k + 2, NU - ENDB + k, false, false, 0);
    STAMP(3 + wave);
    {
      float v[8];
#pragma unroll
      for (int j = 0; j < 4; ++j) { v[2 * j] = acc[j][0]; v[2 * j + 1] = acc[j][1]; }
      float z0, z1;
      xrow_reduce8(v, z0, z1);
      float* rec = &lds[L_REC + wave * 128];
      rec[8 * c + r] = z0 * fw;
      rec[8 * c + 4 + r] = z1 * fw;
    }
    __syncthreads();
  } else {
    const int hw = wave - NS;
    const u32x4 xa = *reinterpret_cast<const u32x4*>(xb + 8 * lane);
    FENCE();
    u32x4 wv[32];
    {
      const u16* pq = wq + (size_t)(128 * hw + 32 * r) * (NH * DH) + h * DH + 8 * c;
#pragma unroll
      for (int i = 0; i < 8; ++i) wv[i] = ldq(pq + (size_t)i * (NH * DH));
      FENCE();
      __syncthreads();
#pragma unroll
      for (int i = 8; i < 32; ++i) wv[i] = ldq(pq + (size_t)i * (NH * DH));
    }
    FENCE();
    unsigned* xn = reinterpret_cast<unsigned*>(&lds[L_XN + hw * 256]);
    {
      float v[8];
#pragma unroll
      for (int j = 0; j < 4; ++j) { v[2 * j] = bf_lo(xa[j]); v[2 * j + 1] = bf_hi(xa[j]); }
      float s = ((v[0] + v[1]) + (v[2] + v[3])) + ((v[4] + v[5]) + (v[6] + v[7]));
      s = xrow_sum(row_sum16(s)) * (1.0f / DM);
      float q2 = 0.f;
#pragma unroll
      for (int j = 0; j < 8; ++j) { const float d = v[j] - s; q2 = fmaf(d, d, q2); }
      q2 = xrow_sum(row_sum16(q2)) * (1.0f / DM);
      const float mean = rnd_bf16(s);
      const float sdev = rnd_bf16(sqrtf(rnd_bf16(rnd_bf16(q2) + 1e-5f)));
      u32x4 o;
#pragma unroll
      for (int j = 0; j < 4; ++j) o[j] = pk_bf16(rnd_bf16(v[2 * j] - mean) / sdev, rnd_bf16(v[2 * j + 1] - mean) / sdev);
      *reinterpret_cast<u32x4*>(xn + 4 * lane) = o;
      asm volatile("s_waitcnt lgkmcnt(0)" ::: "memory");
    }
    {
      float qa[8] = {0, 0, 0, 0, 0, 0, 0, 0};
#pragma unroll
      for (int k = 0; k < 4; ++k) {
        const u32x4 xv = *reinterpret_cast<const u32x4*>(xn + 64 * hw + 16 * r + 4 * k);
#pragma unroll
        for (int i = 0; i < 4; ++i) gemv_pair(wv[8 * k + 2 * i], wv[8 * k + 2 * i + 1], xv[i], qa);
      }
      float z0, z1;
      xrow_reduce8(qa, z0, z1);
      lds[L_Q + hw * 128 + 8 * c + r] = z0;
      lds[L_Q + hw * 128 + 8 * c + 4 + r] = z1;
      LDS_ARRIVE(0);
    }
    FENCE();
    STAMPW(7, 14);
#define HWAIT() VMWAIT(1)
    {
      const int c7 = (lane & 7) < 7 ? (lane & 7) : 6;
      const u16* p1 = w1 + (size_t)(128 * hw + 16 * (lane >> 3)) * FF + h * FS + 8 * c7;
#pragma unroll
      for (int i = 0; i < 16; ++i) { HWAIT(); wv[i] = ldw(p1 + (size_t)i * FF); FENCE(); }
      float ha[8] = {0, 0, 0, 0, 0, 0, 0, 0};
#pragma unroll
      for (int k = 0; k < 2; ++k) {
        const u32x4 xv = *reinterpret_cast<const u32x4*>(xn + 64 * hw + 8 * (lane >> 3) + 4 * k);
#pragma unroll
        for (int i = 0; i < 4; ++i) gemv_pair(wv[8 * k + 2 * i], wv[8 * k + 2 * i + 1], xv[i], ha);
      }
#pragma unroll
      for (int j = 0; j < 8; ++j) ha[j] += dpp<ROR8>(ha[j]);
      float z0, z1;
      xrow_reduce8(ha, z0, z1);
      if (c < 8) { lds[L_H + hw * 64 + 8 * c + r] = z0; lds[L_H + hw * 64 + 8 * c + 4 + r] = z1; }
      LDS_ARRIVE(1);
    }
    FENCE();
    float y[8] = {0, 0, 0, 0, 0, 0, 0, 0};
    {
      u32x4 w2v[14];
      const u16* p2 = w2 + (size_t)(h * FS + 14 * r) * DM + 128 * hw + 8 * c;
#pragma unroll
      for (int i = 0; i < 14; ++i) { HWAIT(); w2v[i] = ldw(p2 + (size_t)i * DM); FENCE(); }
      const int f = 14 * r + (c < 14 ? c : 13);
      const u16 b1v = b1[h * FS + f];
      LDS_WAIT(1, 2);
      float hs = (lds[L_H + f] + lds[L_H + 64 + f]) + (lds[L_H + 128 + f] + lds[L_H + 192 + f]);
      hs = rnd_bf16(rnd_bf16(hs) + bf_lo(b1v));
      const float u = 0.7978845608028654f * fmaf(0.044715f * hs * hs, hs, hs);
      const float g = hs / (1.0f + __builtin_amdgcn_exp2f(-2.0f * LOG2E * u));
      const unsigned gp = pk_bf16(g, dpp<XOR1>(g));
      gemv_bcast2<7>(w2v, gp, y);
    }
    asm volatile("" : "+v"(y[0]), "+v"(y[1]), "+v"(y[2]), "+v"(y[3]), "+v"(y[4]), "+v"(y[5]), "+v"(y[6]), "+v"(y[7]));
    FENCE();
    unsigned wop[16][8];
    {
      const u16* po = wo + (size_t)(h * DH + 32 * r) * DM + 128 * hw + 8 * c;
#pragma unroll
      for (int i = 0; i < 32; ++i) { HWAIT(); wv[i] = ldw(po + (size_t)i * DM); FENCE(); }
#pragma unroll
      for (int i = 0; i < 16; ++i) {
#pragma unroll
        for (int j = 0; j < 4; ++j) {
          wop[i][2 * j] = __builtin_amdgcn_perm(wv[2 * i + 1][j], wv[2 * i][j], 0x05040100u);
          wop[i][2 * j + 1] = __builtin_amdgcn_perm(wv[2 * i + 1][j], wv[2 * i][j], 0x07060302u);
        }
        asm volatile("" : "+v"(wop[i][0]), "+v"(wop[i][1]), "+v"(wop[i][2]), "+v"(wop[i][3]), "+v"(wop[i][4]), "+v"(wop[i][5]), "+v"(wop[i][6]), "+v"(wop[i][7]));
        FENCE();
      }
    }
    const u16 xres = xb[16 * h + c], b2v = b2[16 * h + c];
    const unsigned tag = __hip_atomic_load(epoch, __ATOMIC_RELAXED, __HIP_MEMORY_SCOPE_AGENT) + 1u;
    const unsigned cnt0 = __hip_atomic_load(epoch + 32, __ATOMIC_RELAXED, __HIP_MEMORY_SCOPE_AGENT);
    float base = bf_lo(xres) + bf_lo(b2v);
    unsigned tagv = tag, cntv = cnt0;
    asm volatile("" : "+v"(base), "+v"(tagv), "+v"(cntv));
    FENCE();
    STAMPW(7, 7);
    __syncthreads();
    {
      const float* rec = &lds[L_REC + 2 * lane];
      const f32x2 o2 = (*reinterpret_cast<const f32x2*>(rec) + *reinterpret_cast<const f32x2*>(rec + 128)) +
                       (*reinterpret_cast<const f32x2*>(rec + 256) + *reinterpret_cast<const f32x2*>(rec + 384));
      gemv_bcast<16>(wop, pk_bf16(o2[0], o2[1]), 0, y);
    }
    float z0, z1;
    xrow_reduce8(y, z0, z1);
    STAMPW(4, 11);
    {
      GAS unsigned long long* gp = gran + ((size_t)(b * NH + 8 * hw + (c >> 1)) * NH + h) * 16 + 8 * (c & 1) + r;
      __hip_atomic_store(gp, ((unsigned long long)tagv << 32) | __float_as_uint(z0), __ATOMIC_RELAXED, __HIP_MEMORY_SCOPE_AGENT);
      __hip_atomic_store(gp + 4, ((unsigned long long)tagv << 32) | __float_as_uint(z1), __ATOMIC_RELAXED, __HIP_MEMORY_SCOPE_AGENT);
    }
    if (hw == 0) {
      if (lane == 0) __hip_atomic_fetch_add(epoch + 32, 1u, __ATOMIC_RELAXED, __HIP_MEMORY_SCOPE_AGENT);
      float tot;
#pragma nounroll
      for (unsigned spins = 0; spins < 64u; ++spins) {
        const unsigned arrived = __hip_atomic_load(epoch + 32, __ATOMIC_RELAXED, __HIP_MEMORY_SCOPE_AGENT) - cntv;
        if (__builtin_amdgcn_readfirstlane(arrived) >= (unsigned)POLL_THR) break;
        __builtin_amdgcn_s_sleep(POLL_SLEEP);
      }
      {
        auto rg = __builtin_amdgcn_make_buffer_rsrc((void*)(ws + WS_GRAN + (size_t)(b * NH + h) * NH * 16 * 8), 0, NH * 16 * 8, 0x00020000);
        float v[8];
        unsigned off[8];
#pragma unroll
        for (int k = 0; k < 8; ++k) { v[k] = 0.f; off[k] = lane * 8 + 512 * k; }
#pragma nounroll
        for (unsigned spins = 0;; ++spins) {
          u32x2 gv[8];
#pragma unroll
          for (int k = 0; k < 8; ++k) gv[k] = __builtin_amdgcn_raw_buffer_load_b64(rg, off[k], 0, 16);
          bool ok = true;
#pragma unroll
          for (int k = 0; k < 8; ++k) {
            const bool hit = off[k] < 4096u && gv[k][1] == tagv;
            v[k] = hit ? __uint_as_float(gv[k][0]) : v[k];
            off[k] = hit ? 0x10000u : off[k];
            ok &= off[k] >= 4096u;
          }
          if (__all(ok)) break;
          if (spins > (1u << 18)) { v[0] = __builtin_nanf(""); break; }
          __builtin_amdgcn_s_sleep(1);
        }
        tot = ((v[0] + v[1]) + (v[2] + v[3])) + ((v[4] + v[5]) + (v[6] + v[7]));
      }
      tot = xrow_sum(tot);
      if (lane < 16) out[b * DM + 16 * h + c] = (u16)(pk_bf16(base + tot, 0.f) & 0xffffu);
      if (h == 0 && lane == 0) __hip_atomic_store(epoch, tagv, __ATOMIC_RELAXED, __HIP_MEMORY_SCOPE_AGENT);
    }
  }
}

extern "C" void kernel_launch(void* const* d_in, const int* in_sizes, int n_in, void* d_out, int out_size, void* d_ws, size_t ws_size,
                              hipStream_t stream) {
  const u16* x = (const u16*)d_in[0];
  const u16* K = (const u16*)d_in[1];
  const u16* V = (const u16*)d_in[2];
  const u16* wq = (const u16*)d_in[3];
  const u16* wo = (const u16*)d_in[4];
  const u16* w1 = (const u16*)d_in[6];
  const u16* b1 = (const u16*)d_in[7];
  const u16* w2 = (const u16*)d_in[8];
  const u16* b2 = (const u16*)d_in[9];
  fused_decode<<<NB * NH, NTHR, 0, stream>>>(x, K, V, wq, wo, w1, b1, w2, b2, (u16*)d_out, (unsigned char*)d_ws);
}
```

```cpp
#define STAMPS
#include <cstdio>
#include <vector>
#include <algorithm>
#include <hip/hip_runtime.h>
#include <hip/hip_bf16.h>
#include <stdint.h>

#define FUSED_TAIL 1
#define PRE 10
#define WIN 6
#define HPACE 2
#define POLL_THR 28
#define POLL_SLEEP 8
#define PRIO 1

typedef unsigned u32x4 __attribute__((ext_vector_type(4)));
typedef unsigned u32x2 __attribute__((ext_vector_type(2)));
typedef float f32x2 __attribute__((ext_vector_type(2)));
typedef float f32x4 __attribute__((ext_vector_type(4)));
typedef __bf16 bf16x2 __attribute__((ext_vector_type(2)));
typedef unsigned short u16;
#define GAS __attribute__((address_space(1)))

constexpr int NB = 8, NH = 32, SK = 4096, DH = 128, DM = 512, FF = 1792, FS = FF / NH;
constexpr int NW = 8, NTHR = NW * 64;
constexpr int NS = 4;
constexpr int NU = SK / (16 * NS);
constexpr float SCALE = 0.088388347648318440f, LOG2E = 1.4426950408889634f;

constexpr size_t WS_EPOCH = 0, WS_GRAN = 4096;
constexpr int NSTAMP = 16; constexpr size_t STAMP_OFF = 64u << 20;
#define STAMP(k) do { if (lane == 0) ((volatile unsigned long long*)&stl[0])[k] = __builtin_amdgcn_s_memrealtime(); } while (0)
#define STAMPW(w, k) do { if (wave == (w)) STAMP(k); } while (0)

__device__ __forceinline__ float dot2(unsigned a, unsigned b, float acc) {
  return __builtin_amdgcn_fdot2_f32_bf16(__builtin_bit_cast(bf16x2, a), __builtin_bit_cast(bf16x2, b), acc, false);
}
template <int CTRL> __device__ __forceinline__ float dpp(float x) {
  return __builtin_bit_cast(float, __builtin_amdgcn_mov_dpp(__builtin_bit_cast(int, x), CTRL, 0xf, 0xf, true));
}
constexpr int ROR1 = 0x121, ROR2 = 0x122, ROR4 = 0x124, ROR8 = 0x128, XOR1 = 0xB1, XOR2 = 0x4E;
__device__ __forceinline__ float row_sum16(float x) {
  x += dpp<ROR8>(x); x += dpp<ROR4>(x); x += dpp<ROR2>(x); x += dpp<ROR1>(x); return x;
}
__device__ __forceinline__ float row_max16(float x) {
  x = fmaxf(x, dpp<ROR8>(x)); x = fmaxf(x, dpp<ROR4>(x)); x = fmaxf(x, dpp<ROR2>(x)); x = fmaxf(x, dpp<ROR1>(x)); return x;
}
__device__ __forceinline__ float xrow_sum(float x) {
  auto s = __builtin_amdgcn_permlane16_swap(__float_as_uint(x), __float_as_uint(x), false, false);
  x = __uint_as_float(s[0]) + __uint_as_float(s[1]);
  auto t = __builtin_amdgcn_permlane32_swap(__float_as_uint(x), __float_as_uint(x), false, false);
  return __uint_as_float(t[0]) + __uint_as_float(t[1]);
}
__device__ __forceinline__ float xrow_max(float x) {
  auto s = __builtin_amdgcn_permlane16_swap(__float_as_uint(x), __float_as_uint(x), false, false);
  x = fmaxf(__uint_as_float(s[0]), __uint_as_float(s[1]));
  auto t = __builtin_amdgcn_permlane32_swap(__float_as_uint(x), __float_as_uint(x), false, false);
  return fmaxf(__uint_as_float(t[0]), __uint_as_float(t[1]));
}
__device__ __forceinline__ void xrow_reduce8(const float (&v)[8], float& z0, float& z1) {
  float g[4];
#pragma unroll
  for (int k = 0; k < 4; ++k) {
    auto s = __builtin_amdgcn_permlane16_swap(__float_as_uint(v[2 * k]), __float_as_uint(v[2 * k + 1]), false, false);
    g[k] = __uint_as_float(s[0]) + __uint_as_float(s[1]);
  }
  auto a = __builtin_amdgcn_permlane32_swap(__float_as_uint(g[0]), __float_as_uint(g[1]), false, false);
  z0 = __uint_as_float(a[0]) + __uint_as_float(a[1]);
  auto b = __builtin_amdgcn_permlane32_swap(__float_as_uint(g[2]), __float_as_uint(g[3]), false, false);
  z1 = __uint_as_float(b[0]) + __uint_as_float(b[1]);
}
__device__ __forceinline__ float bf_lo(unsigned u) { return __uint_as_float(u << 16); }
__device__ __forceinline__ float bf_hi(unsigned u) { return __uint_as_float(u & 0xffff0000u); }
__device__ __forceinline__ unsigned pk_bf16(float lo, float hi) {
  bf16x2 r = {(__bf16)lo, (__bf16)hi};
  return __builtin_bit_cast(unsigned, r);
}
__device__ __forceinline__ float rnd_bf16(float x) { return (float)(__bf16)x; }
__device__ __forceinline__ void gemv_pair(const u32x4& a, const u32x4& b, unsigned w, float (&acc)[8]) {
#pragma unroll
  for (int j = 0; j < 4; ++j) {
    acc[2 * j]     = dot2(__builtin_amdgcn_perm(b[j], a[j], 0x05040100u), w, acc[2 * j]);
    acc[2 * j + 1] = dot2(__builtin_amdgcn_perm(b[j], a[j], 0x07060302u), w, acc[2 * j + 1]);
  }
}
#define FENCE() do { asm volatile("" ::: "memory"); __builtin_amdgcn_sched_barrier(0); } while (0)
#define NOW 0
#define WIN0 WIN
#define ENDB 0
__device__ __forceinline__ u32x4 ldq(const u16* p) {
  return __builtin_nontemporal_load(reinterpret_cast<const u32x4*>(p));
}
__device__ __forceinline__ u32x4 ldw(const u16* p) {
  return __builtin_nontemporal_load(reinterpret_cast<const u32x4*>(p));
}
template <int N> __device__ __forceinline__ void vmwait() { asm volatile("s_waitcnt vmcnt(%0)" :: "i"(N) : "memory"); __builtin_amdgcn_sched_barrier(0); }

constexpr int L_S = 0, L_Q = L_S + SK, L_H = L_Q + 4 * 128, L_REC = L_H + 4 * 64, L_ML = L_REC + 4 * 128, L_XN = L_ML + 8,
              L_CNT = L_XN + 4 * 256, L_END = L_CNT + 4;
#define LDS_ARRIVE(i) do { asm volatile("s_waitcnt lgkmcnt(0)" ::: "memory"); \
    if (lane == 0) __hip_atomic_fetch_add(reinterpret_cast<unsigned*>(lds) + L_CNT + (i), 1u, __ATOMIC_RELAXED, __HIP_MEMORY_SCOPE_WORKGROUP); } while (0)
#define LDS_WAIT(i, slp) do { \
    while (__hip_atomic_load(reinterpret_cast<unsigned*>(lds) + L_CNT + (i), __ATOMIC_RELAXED, __HIP_MEMORY_SCOPE_WORKGROUP) < 4u) \
      __builtin_amdgcn_s_sleep(slp); \
    asm volatile("" ::: "memory"); } while (0)
#define STR2(x) #x
#define STR(x) STR2(x)
#define VMWAIT(n) do { asm volatile("s_waitcnt vmcnt(" STR(n) ")" ::: "memory"); __builtin_amdgcn_sched_barrier(0); } while (0)
template <int K> __device__ __forceinline__ unsigned row_bcast(unsigned v) {
  return (unsigned)__builtin_amdgcn_mov_dpp((int)v, 0x150 + K, 0xf, 0xf, true);
}
template <int N, int I = 0> __device__ __forceinline__ void gemv_bcast(const unsigned (&wp)[N][8], unsigned act, int, float (&y)[8]) {
  if constexpr (I < N) {
    const unsigned a = row_bcast<I>(act);
#pragma unroll
    for (int j = 0; j < 8; ++j) y[j] = dot2(wp[I][j], a, y[j]);
    gemv_bcast<N, I + 1>(wp, act, 0, y);
  }
}
template <int N, int I = 0> __device__ __forceinline__ void gemv_bcast2(const u32x4 (&wv)[2 * N], unsigned act, float (&y)[8]) {
  if constexpr (I < N) {
    gemv_pair(wv[2 * I], wv[2 * I + 1], row_bcast<2 * I>(act), y);
    gemv_bcast2<N, I + 1>(wv, act, y);
  }
}

__global__ void __launch_bounds__(NTHR, 2)
fused_decode(const u16* __restrict__ x, const u16* __restrict__ Kg, const u16* __restrict__ Vg, const u16* __restrict__ wq,
             const u16* __restrict__ wo, const u16* __restrict__ w1, const u16* __restrict__ b1, const u16* __restrict__ w2,
             const u16* __restrict__ b2, u16* __restrict__ out, unsigned char* __restrict__ ws) {
  __shared__ __attribute__((aligned(16))) float lds[L_END];
  const int bid = blockIdx.x, h = (bid & 7) * 4 + ((bid >> 3) & 3), b = bid >> 5;
  const int tid = threadIdx.x, lane = tid & 63, wave = __builtin_amdgcn_readfirstlane(tid >> 6), r = lane >> 4, c = lane & 15;
  __shared__ unsigned long long stl[NSTAMP];
  STAMPW(0, 0);
  const u16* xb = x + b * DM;
  GAS unsigned* epoch = (GAS unsigned*)(ws + WS_EPOCH) + 64 * b;
  GAS unsigned long long* gran = (GAS unsigned long long*)(ws + WS_GRAN);

  if (wave < NS) {
    __builtin_amdgcn_s_setprio(1);
    const size_t kvbase = (size_t)(b * NH + h) * SK * DH;
    auto rk = __builtin_amdgcn_make_buffer_rsrc((void*)(Kg + kvbase), 0, SK * DH * 2, 0x00020000);
    auto rv = __builtin_amdgcn_make_buffer_rsrc((void*)(Vg + kvbase), 0, SK * DH * 2, 0x00020000);
    const int rot = (bid >> 3) * 2;
    const unsigned voff = lane * 16 + wave * 4096;
#define LD(rs, u, t) __builtin_amdgcn_raw_buffer_load_b128(rs, voff + 1024 * (t), (((u) + rot) & (NU - 1)) * (NS * 4096), 2)
#define WINWAIT() VMWAIT(5)
    u32x4 R[PRE][4];
#pragma unroll
    for (int n = 0; n < WIN0; ++n) R[n >> 2][n & 3] = LD(rk, n >> 2, n & 3);
    FENCE();
    if (tid < 4) reinterpret_cast<unsigned*>(lds)[L_CNT + tid] = 0u;
    __syncthreads();
#pragma unroll
    for (int n = WIN0; n < 4 * PRE; ++n) { if (n < 16) vmwait<WIN0 - 1>(); else WINWAIT(); R[n >> 2][n & 3] = LD(rk, n >> 2, n & 3); FENCE(); }
    STAMPW(0, 1);
    LDS_WAIT(0, 1);
    STAMPW(0, 2);
    u32x4 qp;
    {
      f32x4 a = {0, 0, 0, 0}, bq = {0, 0, 0, 0};
#pragma unroll
      for (int w = 0; w < 4; ++w) {
        a += *reinterpret_cast<const f32x4*>(&lds[L_Q + w * 128 + 8 * c]);
        bq += *reinterpret_cast<const f32x4*>(&lds[L_Q + w * 128 + 8 * c + 4]);
      }
      qp = u32x4{pk_bf16(a[0], a[1]), pk_bf16(a[2], a[3]), pk_bf16(bq[0], bq[1]), pk_bf16(bq[2], bq[3])};
    }
    float* sw = &lds[L_S + wave * 1024 + r * 4 + (c & 3)];
    const bool b0 = (c & 1) != 0, b1s = (c & 2) != 0;
#define KSTEP(s_, u_, ISSUE_, rs_, nu_) do { \
      float sc[4]; \
      _Pragma("unroll") for (int t = 0; t < 4; ++t) { \
        sc[t] = dot2(R[s_][t][3], qp[3], dot2(R[s_][t][2], qp[2], dot2(R[s_][t][1], qp[1], dot2(R[s_][t][0], qp[0], 0.f)))); \
        asm volatile("" : "+v"(sc[t]) :: "memory"); \
        __builtin_amdgcn_sched_barrier(0); \
        if (ISSUE_) { if (WIN < 8) WINWAIT(); R[s_][t] = LD(rs_, nu_, t); } \
        __builtin_amdgcn_sched_barrier(0); \
      } \
        \
      const float e0 = (b0 ? sc[1] : sc[0]) + dpp<XOR1>(b0 ? sc[0] : sc[1]); \
      const float e1 = (b0 ? sc[3] : sc[2]) + dpp<XOR1>(b0 ? sc[2] : sc[3]); \
      float f = (b1s ? e1 : e0) + dpp<XOR2>(b1s ? e0 : e1); \
      f += dpp<ROR4>(f); f += dpp<ROR8>(f); \
      if (c < 4) sw[(u_) * 16] = f; \
      __builtin_amdgcn_sched_barrier(0); \
    } while (0)
    KSTEP(0, 0, false, rk, 0);
    KSTEP(1, 1, false, rk, 0);
#pragma unroll
    for (int k = 0; k < 8; ++k) {
      WINWAIT(); R[k >> 2][k & 3] = LD(rk, PRE + (k >> 2), k & 3); FENCE();
      if (2 + k < PRE) KSTEP(2 + k, 2 + k, false, rk, 0);
    }
#pragma unroll
    for (int k = 10; k < PRE; ++k) KSTEP(k, k, false, rk, 0);
    static_assert(PRE % 2 == 0 && PRE >= 4, "ring parity");
    for (int u = PRE; u < NU - 2; u += 2) { KSTEP(0, u, true, rk, u + 2); KSTEP(1, u + 1, true, rk, u + 3); }
    KSTEP(0, NU - 2, true, rv, 0); KSTEP(1, NU - 1, true, rv, 1);
    FENCE();
    STAMPW(0, 12); STAMPW(3, 13);
    {
      asm volatile("s_waitcnt lgkmcnt(0)" ::: "memory");
      float* sp = &lds[L_S + wave * 1024 + 4 * lane];
      f32x4 s[4];
#pragma unroll
      for (int k = 0; k < 4; ++k) s[k] = *reinterpret_cast<const f32x4*>(sp + 256 * k);
      FENCE();
      WINWAIT(); R[2][0] = LD(rv, 2, 0); FENCE();
      float m = -INFINITY;
#pragma unroll
      for (int k = 0; k < 4; ++k)
#pragma unroll
        for (int j = 0; j < 4; ++j) { s[k][j] = rnd_bf16(rnd_bf16(s[k][j]) * SCALE); m = fmaxf(m, s[k][j]); }
      asm volatile("" : "+v"(m));
      FENCE();
      WINWAIT(); R[2][1] = LD(rv, 2, 1); FENCE();
      m = xrow_max(row_max16(m));
      float l = 0.f;
#pragma unroll
      for (int k = 0; k < 4; ++k) {
#pragma unroll
        for (int j = 0; j < 4; ++j) { s[k][j] = __builtin_amdgcn_exp2f((s[k][j] - m) * LOG2E); l += s[k][j]; }
        *reinterpret_cast<f32x4*>(sp + 256 * k) = s[k];
        if (k == 1) { FENCE(); WINWAIT(); R[2][2] = LD(rv, 2, 2); FENCE(); }
      }
      FENCE();
      WINWAIT(); R[2][3] = LD(rv, 2, 3); FENCE();
      l = xrow_sum(row_sum16(l));
      if (lane == 0) *reinterpret_cast<f32x2*>(&lds[L_ML + 2 * wave]) = f32x2{m, l};
      LDS_ARRIVE(2);
    }
    f32x2 acc[4] = {{0, 0}, {0, 0}, {0, 0}, {0, 0}};
    const float* pr = &lds[L_S + wave * 1024 + r * 4];
    f32x4 pc = *reinterpret_cast<const f32x4*>(pr);
#define VSTEP(s_, u_, ISSUE_, PACE_, nu_) do { \
      const f32x4 pn = *reinterpret_cast<const f32x4*>(pr + (((u_) + 1) & (NU - 1)) * 16); \
      _Pragma("unroll") for (int t = 0; t < 4; ++t) { \
        _Pragma("unroll") for (int j = 0; j < 4; ++j) { \
          const f32x2 vv = {bf_lo(R[s_][t][j]), bf_hi(R[s_][t][j])}; \
          acc[j] = __builtin_elementwise_fma(vv, f32x2{pc[t], pc[t]}, acc[j]); } \
        asm volatile("" : "+v"(acc[0]), "+v"(acc[1]), "+v"(acc[2]), "+v"(acc[3]) :: "memory"); \
        __builtin_amdgcn_sched_barrier(0); \
        if (ISSUE_) { if (PACE_) WINWAIT(); R[s_][t] = LD(rv, nu_, t); } \
        __builtin_amdgcn_sched_barrier(0); \
      } \
      pc = pn; \
    } while (0)
    VSTEP(0, 0, true, true, 3); VSTEP(1, 1, false, false, 0);
    for (int u = 2; u < NU - 2 - ENDB; u += 2) { VSTEP(2, u, true, WIN < 8, u + 2); VSTEP(0, u + 1, true, WIN < 8, u + 3); }
#pragma unroll
    for (int k = 0; k < ENDB; ++k) {
#pragma unroll
      for (int t = 0; t < 4; ++t) R[k == 0 ? 1 : k + 2][t] = LD(rv, NU - ENDB + k, t);
    }
    FENCE();
    float fw;
    {
      LDS_WAIT(2, 1);
      const f32x2 ml = *reinterpret_cast<const f32x2*>(&lds[L_ML + 2 * (lane & 3)]);
      float M = fmaxf(ml[0], dpp<XOR1>(ml[0])); M = fmaxf(M, dpp<XOR2>(M));
      const float e = __builtin_amdgcn_exp2f((ml[0] - M) * LOG2E);
      float L = ml[1] * e; L += dpp<XOR1>(L); L += dpp<XOR2>(L);
      fw = __uint_as_float(__builtin_amdgcn_readlane(__float_as_uint(e / L), wave));
    }
    FENCE();
    VSTEP(2, NU - 2 - ENDB, false, false, 0); VSTEP(0, NU - 1 - ENDB, false, false, 0);
#pragma unroll
    for (int k = 0; k < ENDB; ++k) VSTEP(k == 0 ? 1 : k + 2, NU - ENDB + k, false, false, 0);
    STAMP(3 + wave);
    {
      float v[8];
#pragma unroll
      for (int j = 0; j < 4; ++j) { v[2 * j] = acc[j][0]; v[2 * j + 1] = acc[j][1]; }
      float z0, z1;
      xrow_reduce8(v, z0, z1);
      float* rec = &lds[L_REC + wave * 128];
      rec[8 * c + r] = z0 * fw;
      rec[8 * c + 4 + r] = z1 * fw;
    }
    __syncthreads();
  } else {
    const int hw = wave - NS;
    const u32x4 xa = *reinterpret_cast<const u32x4*>(xb + 8 * lane);
    FENCE();
    u32x4 wv[32];
    {
      const u16* pq = wq + (size_t)(128 * hw + 32 * r) * (NH * DH) + h * DH + 8 * c;
#pragma unroll
      for (int i = 0; i < 8; ++i) wv[i] = ldq(pq + (size_t)i * (NH * DH));
      FENCE();
      __syncthreads();
#pragma unroll
      for (int i = 8; i < 32; ++i) wv[i] = ldq(pq + (size_t)i * (NH * DH));
    }
    FENCE();
    unsigned* xn = reinterpret_cast<unsigned*>(&lds[L_XN + hw * 256]);
    {
      float v[8];
#pragma unroll
      for (int j = 0; j < 4; ++j) { v[2 * j] = bf_lo(xa[j]); v[2 * j + 1] = bf_hi(xa[j]); }
      float s = ((v[0] + v[1]) + (v[2] + v[3])) + ((v[4] + v[5]) + (v[6] + v[7]));
      s = xrow_sum(row_sum16(s)) * (1.0f / DM);
      float q2 = 0.f;
#pragma unroll
      for (int j = 0; j < 8; ++j) { const float d = v[j] - s; q2 = fmaf(d, d, q2); }
      q2 = xrow_sum(row_sum16(q2)) * (1.0f / DM);
      const float mean = rnd_bf16(s);
      const float sdev = rnd_bf16(sqrtf(rnd_bf16(rnd_bf16(q2) + 1e-5f)));
      u32x4 o;
#pragma unroll
      for (int j = 0; j < 4; ++j) o[j] = pk_bf16(rnd_bf16(v[2 * j] - mean) / sdev, rnd_bf16(v[2 * j + 1] - mean) / sdev);
      *reinterpret_cast<u32x4*>(xn + 4 * lane) = o;
      asm volatile("s_waitcnt lgkmcnt(0)" ::: "memory");
    }
    {
      float qa[8] = {0, 0, 0, 0, 0, 0, 0, 0};
#pragma unroll
      for (int k = 0; k < 4; ++k) {
        const u32x4 xv = *reinterpret_cast<const u32x4*>(xn + 64 * hw + 16 * r + 4 * k);
#pragma unroll
        for (int i = 0; i < 4; ++i) gemv_pair(wv[8 * k + 2 * i], wv[8 * k + 2 * i + 1], xv[i], qa);
      }
      float z0, z1;
      xrow_reduce8(qa, z0, z1);
      lds[L_Q + hw * 128 + 8 * c + r] = z0;
      lds[L_Q + hw * 128 + 8 * c + 4 + r] = z1;
      LDS_ARRIVE(0);
    }
    FENCE();
    STAMPW(7, 14);
#define HWAIT() VMWAIT(1)
    {
      const int c7 = (lane & 7) < 7 ? (lane & 7) : 6;
      const u16* p1 = w1 + (size_t)(128 * hw + 16 * (lane >> 3)) * FF + h * FS + 8 * c7;
#pragma unroll
      for (int i = 0; i < 16; ++i) { HWAIT(); wv[i] = ldw(p1 + (size_t)i * FF); FENCE(); }
      float ha[8] = {0, 0, 0, 0, 0, 0, 0, 0};
#pragma unroll
      for (int k = 0; k < 2; ++k) {
        const u32x4 xv = *reinterpret_cast<const u32x4*>(xn + 64 * hw + 8 * (lane >> 3) + 4 * k);
#pragma unroll
        for (int i = 0; i < 4; ++i) gemv_pair(wv[8 * k + 2 * i], wv[8 * k + 2 * i + 1], xv[i], ha);
      }
#pragma unroll
      for (int j = 0; j < 8; ++j) ha[j] += dpp<ROR8>(ha[j]);
      float z0, z1;
      xrow_reduce8(ha, z0, z1);
      if (c < 8) { lds[L_H + hw * 64 + 8 * c + r] = z0; lds[L_H + hw * 64 + 8 * c + 4 + r] = z1; }
      LDS_ARRIVE(1);
    }
    FENCE();
    float y[8] = {0, 0, 0, 0, 0, 0, 0, 0};
    {
      u32x4 w2v[14];
      const u16* p2 = w2 + (size_t)(h * FS + 14 * r) * DM + 128 * hw + 8 * c;
#pragma unroll
      for (int i = 0; i < 14; ++i) { HWAIT(); w2v[i] = ldw(p2 + (size_t)i * DM); FENCE(); }
      const int f = 14 * r + (c < 14 ? c : 13);
      const u16 b1v = b1[h * FS + f];
      LDS_WAIT(1, 2);
      float hs = (lds[L_H + f] + lds[L_H + 64 + f]) + (lds[L_H + 128 + f] + lds[L_H + 192 + f]);
      hs = rnd_bf16(rnd_bf16(hs) + bf_lo(b1v));
      const float u = 0.7978845608028654f * fmaf(0.044715f * hs * hs, hs, hs);
      const float g = hs / (1.0f + __builtin_amdgcn_exp2f(-2.0f * LOG2E * u));
      const unsigned gp = pk_bf16(g, dpp<XOR1>(g));
      gemv_bcast2<7>(w2v, gp, y);
    }
    asm volatile("" : "+v"(y[0]), "+v"(y[1]), "+v"(y[2]), "+v"(y[3]), "+v"(y[4]), "+v"(y[5]), "+v"(y[6]), "+v"(y[7]));
    FENCE();
    unsigned wop[16][8];
    {
      const u16* po = wo + (size_t)(h * DH + 32 * r) * DM + 128 * hw + 8 * c;
#pragma unroll
      for (int i = 0; i < 32; ++i) { HWAIT(); wv[i] = ldw(po + (size_t)i * DM); FENCE(); }
#pragma unroll
      for (int i = 0; i < 16; ++i) {
#pragma unroll
        for (int j = 0; j < 4; ++j) {
          wop[i][2 * j] = __builtin_amdgcn_perm(wv[2 * i + 1][j], wv[2 * i][j], 0x05040100u);
          wop[i][2 * j + 1] = __builtin_amdgcn_perm(wv[2 * i + 1][j], wv[2 * i][j], 0x07060302u);
        }
        asm volatile("" : "+v"(wop[i][0]), "+v"(wop[i][1]), "+v"(wop[i][2]), "+v"(wop[i][3]), "+v"(wop[i][4]), "+v"(wop[i][5]), "+v"(wop[i][6]), "+v"(wop[i][7]));
        FENCE();
      }
    }
    const u16 xres = xb[16 * h + c], b2v = b2[16 * h + c];
    const unsigned tag = __hip_atomic_load(epoch, __ATOMIC_RELAXED, __HIP_MEMORY_SCOPE_AGENT) + 1u;
    const unsigned cnt0 = __hip_atomic_load(epoch + 32, __ATOMIC_RELAXED, __HIP_MEMORY_SCOPE_AGENT);
    float base = bf_lo(xres) + bf_lo(b2v);
    unsigned tagv = tag, cntv = cnt0;
    asm volatile("" : "+v"(base), "+v"(tagv), "+v"(cntv));
    FENCE();
    STAMPW(7, 7);
    __syncthreads();
    {
      const float* rec = &lds[L_REC + 2 * lane];
      const f32x2 o2 = (*reinterpret_cast<const f32x2*>(rec) + *reinterpret_cast<const f32x2*>(rec + 128)) +
                       (*reinterpret_cast<const f32x2*>(rec + 256) + *reinterpret_cast<const f32x2*>(rec + 384));
      gemv_bcast<16>(wop, pk_bf16(o2[0], o2[1]), 0, y);
    }
    float z0, z1;
    xrow_reduce8(y, z0, z1);
    STAMPW(4, 11);
    {
      GAS unsigned long long* gp = gran + ((size_t)(b * NH + 8 * hw + (c >> 1)) * NH + h) * 16 + 8 * (c & 1) + r;
      __hip_atomic_store(gp, ((unsigned long long)tagv << 32) | __float_as_uint(z0), __ATOMIC_RELAXED, __HIP_MEMORY_SCOPE_AGENT);
      __hip_atomic_store(gp + 4, ((unsigned long long)tagv << 32) | __float_as_uint(z1), __ATOMIC_RELAXED, __HIP_MEMORY_SCOPE_AGENT);
    }
    if (hw == 0) {
      if (lane == 0) __hip_atomic_fetch_add(epoch + 32, 1u, __ATOMIC_RELAXED, __HIP_MEMORY_SCOPE_AGENT);
      float tot;
#pragma nounroll
      for (unsigned spins = 0; spins < 64u; ++spins) {
        const unsigned arrived = __hip_atomic_load(epoch + 32, __ATOMIC_RELAXED, __HIP_MEMORY_SCOPE_AGENT) - cntv;
        if (__builtin_amdgcn_readfirstlane(arrived) >= (unsigned)POLL_THR) break;
        __builtin_amdgcn_s_sleep(POLL_SLEEP);
      }
      {
        auto rg = __builtin_amdgcn_make_buffer_rsrc((void*)(ws + WS_GRAN + (size_t)(b * NH + h) * NH * 16 * 8), 0, NH * 16 * 8, 0x00020000);
        float v[8];
        unsigned off[8];
#pragma unroll
        for (int k = 0; k < 8; ++k) { v[k] = 0.f; off[k] = lane * 8 + 512 * k; }
#pragma nounroll
        for (unsigned spins = 0;; ++spins) {
          u32x2 gv[8];
#pragma unroll
          for (int k = 0; k < 8; ++k) gv[k] = __builtin_amdgcn_raw_buffer_load_b64(rg, off[k], 0, 16);
          bool ok = true;
#pragma unroll
          for (int k = 0; k < 8; ++k) {
            const bool hit = off[k] < 4096u && gv[k][1] == tagv;
            v[k] = hit ? __uint_as_float(gv[k][0]) : v[k];
            off[k] = hit ? 0x10000u : off[k];
            ok &= off[k] >= 4096u;
          }
          if (__all(ok)) break;
          if (spins > (1u << 18)) { v[0] = __builtin_nanf(""); break; }
          __builtin_amdgcn_s_sleep(1);
        }
        tot = ((v[0] + v[1]) + (v[2] + v[3])) + ((v[4] + v[5]) + (v[6] + v[7]));
      }
      tot = xrow_sum(tot);
      if (lane < 16) out[b * DM + 16 * h + c] = (u16)(pk_bf16(base + tot, 0.f) & 0xffffu);
      if (h == 0 && lane == 0) __hip_atomic_store(epoch, tagv, __ATOMIC_RELAXED, __HIP_MEMORY_SCOPE_AGENT);
    }
    if (wave == 4) { STAMP(15); asm volatile("s_waitcnt lgkmcnt(0)" ::: "memory"); if (lane < NSTAMP) ((unsigned long long*)(ws + STAMP_OFF))[bid * NSTAMP + lane] = stl[lane]; }
  }
}

__global__ void diag_fill(u32x4* p, size_t n, unsigned seed) {
  size_t i = blockIdx.x * (size_t)blockDim.x + threadIdx.x;
  for (; i < n; i += (size_t)gridDim.x * blockDim.x) { unsigned h = (unsigned)i * 2654435761u + seed; u32x4 v = {h, h * 31u + 7u, h ^ 0x9e3779b9u, h * 17u}; p[i] = v; }
}
extern "C" void kernel_launch(void* const* d_in, const int* in_sizes, int n_in, void* d_out, int out_size, void* d_ws, size_t ws_size,
                              hipStream_t stream) {
  const u16* x = (const u16*)d_in[0];
  const u16* K = (const u16*)d_in[1];
  const u16* V = (const u16*)d_in[2];
  const u16* wq = (const u16*)d_in[3];
  const u16* wo = (const u16*)d_in[4];
  const u16* w1 = (const u16*)d_in[6];
  const u16* b1 = (const u16*)d_in[7];
  const u16* w2 = (const u16*)d_in[8];
  const u16* b2 = (const u16*)d_in[9];
  hipStreamCaptureStatus cs = hipStreamCaptureStatusNone;
  hipStreamIsCapturing(stream, &cs);
  fprintf(stderr, "DIAG cap %d x %p K %p V %p wq %p wo %p w1 %p w2 %p out %p ws %p\n", (int)cs, x, K, V, wq, wo, w1, w2, d_out, d_ws);
  if (cs == hipStreamCaptureStatusNone) {
    std::vector<unsigned long long> hs(256 * NSTAMP);
    for (int mode = 0; mode < 2; ++mode) {
      std::vector<double> ik; double st[NSTAMP] = {0};
      for (int rep = 0; rep < 15; ++rep) {
        if (mode == 0) diag_fill<<<2048, 256, 0, stream>>>((u32x4*)d_ws, ws_size / 16, rep);
        fused_decode<<<NB * NH, NTHR, 0, stream>>>(x, K, V, wq, wo, w1, b1, w2, b2, (u16*)d_out, (unsigned char*)d_ws);
        hipStreamSynchronize(stream);
        hipMemcpy(hs.data(), (char*)d_ws + STAMP_OFF, 256 * NSTAMP * 8, hipMemcpyDeviceToHost);
        unsigned long long mn = ~0ull, mx = 0;
        for (int i = 0; i < 256; ++i) { mn = std::min(mn, hs[i * NSTAMP]); mx = std::max(mx, hs[i * NSTAMP + NSTAMP - 1]); }
        ik.push_back((mx - mn) * 0.01);
        for (int k = 0; k < NSTAMP; ++k) for (int i = 0; i < 256; ++i) st[k] += (double)(long long)(hs[i * NSTAMP + k] - mn) * 0.01 / 256 / 15;
      }
      std::sort(ik.begin(), ik.end());
      fprintf(stderr, "DIAG mode %d in-kernel med %.1f min %.1f max %.1f | st1 %.1f st2 %.1f st12 %.1f st3 %.1f st11 %.1f st14 %.1f st7 %.1f\n", mode, ik[7], ik[0], ik[14], st[1], st[2], st[12], st[3], st[11], st[14], st[7]);
    }
  }
  fused_decode<<<NB * NH, NTHR, 0, stream>>>(x, K, V, wq, wo, w1, b1, w2, b2, (u16*)d_out, (unsigned char*)d_ws);
}
```

```cpp
#include <hip/hip_runtime.h>
#include <hip/hip_bf16.h>
#include <stdint.h>

constexpr int PRE = 7;
constexpr int WIN = 6, NSTEP = 5;
constexpr unsigned LSTEP = 60;
constexpr unsigned NBOOST = 16;
constexpr unsigned TAU = 3545, OFFS = 100;
constexpr unsigned QN = 12, QGAP = 2000;
constexpr unsigned PFN = 16, PFD = 3, PFL = 3, PFGAP = 2700;
constexpr unsigned JUMP = 120;
constexpr int HPACE = 2, QPACE = 8;
constexpr int PDELAY = 20, QDELAY = 24, QSLP = 8;

typedef unsigned u32x4 __attribute__((ext_vector_type(4)));
typedef unsigned u32x2 __attribute__((ext_vector_type(2)));
typedef float f32x2 __attribute__((ext_vector_type(2)));
typedef float f32x4 __attribute__((ext_vector_type(4)));
typedef __bf16 bf16x2 __attribute__((ext_vector_type(2)));
typedef unsigned short u16;
#define GAS __attribute__((address_space(1)))
#define LAS __attribute__((address_space(3)))

constexpr int NB = 8, NH = 32, SK = 4096, DH = 128, DM = 512, FF = 1792, FS = 64;
constexpr int NW = 12, NTHR = NW * 64;
constexpr int NC = 4;
constexpr int NSLOT = SK / 16 / NC;
constexpr int RING = 8;
constexpr float SCALE = 0.088388347648318440f, LOG2E = 1.4426950408889634f;

constexpr size_t WS_EPOCH = 0, WS_GRAN = 4096, WS_QX = WS_GRAN + (1u << 20);

__device__ __forceinline__ float dot2(unsigned a, unsigned b, float acc) {
  return __builtin_amdgcn_fdot2_f32_bf16(__builtin_bit_cast(bf16x2, a), __builtin_bit_cast(bf16x2, b), acc, false);
}
template <int CTRL> __device__ __forceinline__ float dpp(float x) {
  return __builtin_bit_cast(float, __builtin_amdgcn_mov_dpp(__builtin_bit_cast(int, x), CTRL, 0xf, 0xf, true));
}
constexpr int ROR1 = 0x121, ROR2 = 0x122, ROR4 = 0x124, ROR8 = 0x128, XOR1 = 0xB1, XOR2 = 0x4E;
__device__ __forceinline__ float row_sum16(float x) {
  x += dpp<ROR8>(x); x += dpp<ROR4>(x); x += dpp<ROR2>(x); x += dpp<ROR1>(x); return x;
}
__device__ __forceinline__ float xrow_sum(float x) {
  auto s = __builtin_amdgcn_permlane16_swap(__float_as_uint(x), __float_as_uint(x), false, false);
  x = __uint_as_float(s[0]) + __uint_as_float(s[1]);
  auto t = __builtin_amdgcn_permlane32_swap(__float_as_uint(x), __float_as_uint(x), false, false);
  return __uint_as_float(t[0]) + __uint_as_float(t[1]);
}
__device__ __forceinline__ float half_sum(float x) {
  x = row_sum16(x);
  auto s = __builtin_amdgcn_permlane16_swap(__float_as_uint(x), __float_as_uint(x), false, false);
  return __uint_as_float(s[0]) + __uint_as_float(s[1]);
}
__device__ __forceinline__ float xrow_max(float x) {
  auto s = __builtin_amdgcn_permlane16_swap(__float_as_uint(x), __float_as_uint(x), false, false);
  x = fmaxf(__uint_as_float(s[0]), __uint_as_float(s[1]));
  auto t = __builtin_amdgcn_permlane32_swap(__float_as_uint(x), __float_as_uint(x), false, false);
  return fmaxf(__uint_as_float(t[0]), __uint_as_float(t[1]));
}
__device__ __forceinline__ void xrow_reduce8(const float (&v)[8], float& z0, float& z1) {
  float g[4];
#pragma unroll
  for (int k = 0; k < 4; ++k) {
    auto s = __builtin_amdgcn_permlane16_swap(__float_as_uint(v[2 * k]), __float_as_uint(v[2 * k + 1]), false, false);
    g[k] = __uint_as_float(s[0]) + __uint_as_float(s[1]);
  }
  auto a = __builtin_amdgcn_permlane32_swap(__float_as_uint(g[0]), __float_as_uint(g[1]), false, false);
  z0 = __uint_as_float(a[0]) + __uint_as_float(a[1]);
  auto b = __builtin_amdgcn_permlane32_swap(__float_as_uint(g[2]), __float_as_uint(g[3]), false, false);
  z1 = __uint_as_float(b[0]) + __uint_as_float(b[1]);
}
__device__ __forceinline__ float bf_lo(unsigned u) { return __uint_as_float(u << 16); }
__device__ __forceinline__ float bf_hi(unsigned u) { return __uint_as_float(u & 0xffff0000u); }
__device__ __forceinline__ unsigned pk_bf16(float lo, float hi) {
  bf16x2 r = {(__bf16)lo, (__bf16)hi};
  return __builtin_bit_cast(unsigned, r);
}
__device__ __forceinline__ float rnd_bf16(float x) { return (float)(__bf16)x; }
__device__ __forceinline__ void gemv_pair(const u32x4& a, const u32x4& b, unsigned w, float (&acc)[8]) {
#pragma unroll
  for (int j = 0; j < 4; ++j) {
    acc[2 * j]     = dot2(__builtin_amdgcn_perm(b[j], a[j], 0x05040100u), w, acc[2 * j]);
    acc[2 * j + 1] = dot2(__builtin_amdgcn_perm(b[j], a[j], 0x07060302u), w, acc[2 * j + 1]);
  }
}
#define FENCE() do { asm volatile("" ::: "memory"); __builtin_amdgcn_sched_barrier(0); } while (0)
__device__ __forceinline__ u32x4 ldw(const u16* p) {
  return *reinterpret_cast<const u32x4*>(p);
}
template <int N> __device__ __forceinline__ void vmwait() { asm volatile("s_waitcnt vmcnt(%0)" :: "i"(N) : "memory"); __builtin_amdgcn_sched_barrier(0); }
#define LGKM0() asm volatile("s_waitcnt lgkmcnt(0)" ::: "memory")
template <int K> __device__ __forceinline__ unsigned row_bcast(unsigned v) {
  return (unsigned)__builtin_amdgcn_mov_dpp((int)v, 0x150 + K, 0xf, 0xf, true);
}
template <int N, int I = 0> __device__ __forceinline__ void gemv_bcast(const unsigned (&wp)[N][8], unsigned act, float (&y)[8]) {
  if constexpr (I < N) {
    const unsigned a = row_bcast<I>(act);
#pragma unroll
    for (int j = 0; j < 8; ++j) y[j] = dot2(wp[I][j], a, y[j]);
    gemv_bcast<N, I + 1>(wp, act, y);
  }
}
template <int N, int I = 0> __device__ __forceinline__ void gemv_bcast2(const u32x4 (&wv)[32], unsigned act, float (&y)[8]) {
  if constexpr (I < N) {
    gemv_pair(wv[2 * I], wv[2 * I + 1], row_bcast<I>(act), y);
    gemv_bcast2<N, I + 1>(wv, act, y);
  }
}
template <int W> __device__ __forceinline__ void dma_slot(u32x4 srd, unsigned voff, unsigned soff, unsigned ldsaddr) {
  unsigned keep;
  asm volatile("s_mov_b32 %0, m0\n\ts_mov_b32 m0, %4\n\t"
               "s_waitcnt vmcnt(%5)\n\tbuffer_load_dwordx4 %1, %2, %3 offen nt lds\n\t"
               "s_waitcnt vmcnt(%5)\n\tbuffer_load_dwordx4 %1, %2, %3 offen offset:1024 nt lds\n\t"
               "s_waitcnt vmcnt(%5)\n\tbuffer_load_dwordx4 %1, %2, %3 offen offset:2048 nt lds\n\t"
               "s_waitcnt vmcnt(%5)\n\tbuffer_load_dwordx4 %1, %2, %3 offen offset:3072 nt lds\n\t"
               "s_mov_b32 m0, %0"
               : "=&s"(keep) : "v"(voff), "s"(srd), "s"(soff), "s"(ldsaddr), "i"(W - 1) : "memory");
}

template <int I> __device__ __forceinline__ void dma_pick(unsigned lag, u32x4 srd, unsigned voff, unsigned soff, unsigned ldsaddr, unsigned n, unsigned& pub) {
  if (I == NSTEP - 1 || lag < (I + 1) * LSTEP * 256u) {
    dma_slot<WIN + I>(srd, voff, soff, ldsaddr);
    pub = max(pub + (WIN + I + 3) / 4, n + 1) - (WIN + I + 3) / 4;
  } else if constexpr (I < NSTEP - 1) dma_pick<I + 1>(lag, srd, voff, soff, ldsaddr, n, pub);
}

constexpr int L_RING = 0, L_S = NC * RING * 1024, L_QP = L_S + SK, L_Q = L_QP + 4 * 128, L_H = L_Q + 64, L_REC = L_H + 4 * 64,
              L_XN = L_REC + 2 * NC * 128, L_CTL = L_XN + 4 * 128, L_END = L_CTL + 16;
constexpr int C_DONE = 0, C_LANDED = 4, C_M = 8, C_QP = 12, C_Q = 13, C_H = 14, C_K = 15;
constexpr unsigned XSPIN_CAP = 1u << 18, SPIN_CAP = 1u << 24, OOB = 0x40000000u;
#define ARRIVE(i) do { LGKM0(); \
    if (lane == 0) __hip_atomic_fetch_add((LAS unsigned*)ctl + (i), 1u, __ATOMIC_RELAXED, __HIP_MEMORY_SCOPE_WORKGROUP); } while (0)
#define AWAIT(i, n, slp) do { \
    _Pragma("nounroll") for (unsigned sp_ = 0; __builtin_amdgcn_readfirstlane(ctl[i]) < (unsigned)(n) && sp_ < SPIN_CAP; ++sp_) __builtin_amdgcn_s_sleep(slp); \
    asm volatile("" ::: "memory"); } while (0)
#define ROW_MAX() fmaxf(fmaxf(__uint_as_float(ctl[C_M]), __uint_as_float(ctl[C_M + 1])), fmaxf(__uint_as_float(ctl[C_M + 2]), __uint_as_float(ctl[C_M + 3])))
#define PV_PIECE(T_, p_) do { _Pragma("unroll") for (int j = 0; j < 4; ++j) { \
      const f32x2 vv = {bf_lo((T_)[j]), bf_hi((T_)[j])}; \
      acc[j] = __builtin_elementwise_fma(vv, f32x2{(p_), (p_)}, acc[j]); } } while (0)
#define PV_STORE(rec_) do { float v[8]; \
      _Pragma("unroll") for (int j = 0; j < 4; ++j) { v[2 * j] = acc[j][0]; v[2 * j + 1] = acc[j][1]; } \
      float z0, z1; xrow_reduce8(v, z0, z1); \
      *reinterpret_cast<f32x2*>(&lds[L_REC + (rec_) * 128 + 2 * lane]) = f32x2{z0, z1};       } while (0)

__global__ void __launch_bounds__(NTHR, 3)
fused_decode(const u16* __restrict__ x, const u16* __restrict__ Kg, const u16* __restrict__ Vg, const u16* __restrict__ wq,
             unsigned char* __restrict__ ws, const u16* __restrict__ wo, const u16* __restrict__ w1, const u16* __restrict__ w2,
             const u16* __restrict__ b1, const u16* __restrict__ b2, u16* __restrict__ out) {
  __shared__ __attribute__((aligned(16))) float lds[L_END];
  const int bid = blockIdx.x, h = (bid & 7) * 4 + ((bid >> 3) & 3), b = bid >> 5;
  const int tid = threadIdx.x, lane = tid & 63, wave = __builtin_amdgcn_readfirstlane(tid >> 6), r = lane >> 4, c = lane & 15;
  volatile LAS unsigned* ctl = (volatile LAS unsigned*)&lds[L_CTL];

  if (tid < 16) ctl[tid] = 0u;
  const size_t kvbase = (size_t)(b * NH + h) * SK * DH;
  const unsigned long long ka = (unsigned long long)(Kg + kvbase), va = (unsigned long long)(Vg + kvbase);
  const u32x4 rk = {(unsigned)ka, (unsigned)(ka >> 32) & 0xffffu, SK * DH * 2, 0x00020000u};
  const u32x4 rv = {(unsigned)va, (unsigned)(va >> 32) & 0xffffu, SK * DH * 2, 0x00020000u};
  const unsigned lbase = (unsigned)(size_t)(LAS float*)&lds[L_RING] + wave * (RING * 4096);
  const unsigned voff = lane * 16 + wave * 4096;
  const unsigned rot = (4 * b + 2 * (h & 1) + ((h >> 1) & 1)) * 2;
  asm volatile("" :: "s"(x), "s"(wq), "s"(Vg), "s"(ws));
  LGKM0();
  unsigned tbase = 0;
  if (wave < NC) {
    __builtin_amdgcn_s_setprio(3);
    dma_slot<64>(rk, voff, rot * (NC * 4096), lbase);
    dma_slot<64>(rk, voff, (rot + 1) * (NC * 4096), lbase + 4096);
    tbase = (unsigned)__builtin_amdgcn_s_memrealtime();
    asm volatile("" : "+s"(tbase));
  }
  __syncthreads();
  constexpr unsigned NRING = 2 * NSLOT - 1;

  if (wave < NC) {
    unsigned limit = RING, pub = 0, plag = 0;
    unsigned due = (tbase << 8) + OFFS * 256 + wave * TAU;
#define WAIT_DUE(lag_) do { _Pragma("nounroll") for (int sp = 0; sp < 4000; ++sp) { \
        lag_ = ((unsigned)__builtin_amdgcn_s_memrealtime() << 8) - due; if ((int)lag_ >= 0) break; } } while (0)
#pragma nounroll
    for (unsigned n = 2; n < NRING; ++n) {
      if (n >= limit) {
#pragma nounroll
        for (unsigned sp = 0; sp < SPIN_CAP; ++sp) {
          limit = __builtin_amdgcn_readfirstlane(ctl[C_DONE + wave]) + RING;
          if (n < limit) break;
          if (pub < n) { vmwait<0>(); pub = n; if (lane == 0) ctl[C_LANDED + wave] = pub; }
          __builtin_amdgcn_s_sleep(1);
        }
        asm volatile("" ::: "memory");
      }
      unsigned lag;
      WAIT_DUE(lag);
      if (n >= NBOOST && (int)(lag - plag) > (int)(JUMP * 256u)) { due += lag - plag; lag = plag; }
      plag = lag;
      due += 4 * TAU + (n < QN ? QGAP : n - (PFN + PFL) < 4 * PFD ? PFGAP : 0u);
      dma_pick<0>(n < NBOOST ? 0u : lag, n < NSLOT ? rk : rv, voff, ((n + rot) & (NSLOT - 1)) * (NC * 4096), lbase + (n & (RING - 1)) * 4096, n, pub);
      if (lane == 0) ctl[C_LANDED + wave] = pub;
    }
    {
      AWAIT(C_K, NC, 1);
      f32x4 p = *reinterpret_cast<const f32x4*>(&lds[L_S + wave * 1024 + (NSLOT - 1) * 16 + r * 4]);
      const float M2 = ROW_MAX();
#pragma unroll
      for (int t = 0; t < 4; ++t) p[t] = __builtin_amdgcn_exp2f((p[t] - M2) * LOG2E);
      asm volatile("" : "+v"(p) :: "memory");
      auto rl = __builtin_amdgcn_make_buffer_rsrc((void*)(Vg + kvbase), 0, SK * DH * 2, 0x00020000);
      unsigned lag;
      WAIT_DUE(lag);
      FENCE();
      u32x4 T[4];
#pragma unroll
      for (int t = 0; t < 4; ++t) T[t] = __builtin_amdgcn_raw_buffer_load_b128(rl, voff + 1024 * t, ((NSLOT - 1 + rot) & (NSLOT - 1)) * (NC * 4096), 2);
      FENCE();
      if (lane == 0) {
        vmwait<12>(); ctl[C_LANDED + wave] = max(pub, NRING - 2u);
        vmwait<8>(); ctl[C_LANDED + wave] = max(pub, NRING - 1u);
        vmwait<4>(); ctl[C_LANDED + wave] = NRING;
      }
      FENCE();
      f32x2 acc[4] = {{0, 0}, {0, 0}, {0, 0}, {0, 0}};
#pragma unroll
      for (int t = 0; t < 4; ++t) PV_PIECE(T[t], p[t]);
      PV_STORE(NC + wave);
    }
    __syncthreads();
    return;
  }
  if (wave < 2 * NC) {
    const unsigned ci = wave - NC;
    unsigned seen = 0;
    const LAS u32x4* ring = (const LAS u32x4*)&lds[L_RING] + ci * (RING * 256) + lane;
#define TAKE(n_, R_) do { \
      _Pragma("nounroll") for (unsigned sp_ = 0; seen <= (n_) && sp_ < SPIN_CAP; ++sp_) { \
        seen = __builtin_amdgcn_readfirstlane(ctl[C_LANDED + ci]); \
        if (seen <= (n_)) __builtin_amdgcn_s_sleep(1); } \
      asm volatile("" ::: "memory"); \
      _Pragma("unroll") for (int t = 0; t < 4; ++t) R_[t] = ring[((n_) & (RING - 1)) * 256 + 64 * t]; \
      LGKM0(); \
      if (lane == 0) ctl[C_DONE + ci] = (n_) + 1; \
      asm volatile("" : "+v"(R_[0]), "+v"(R_[1]), "+v"(R_[2]), "+v"(R_[3]) :: "memory"); \
    } while (0)
    u32x4 R[PRE][4];
#pragma unroll
    for (int i = 0; i < PRE; ++i) {
      TAKE((unsigned)i, R[i]);
      if (i == 3) {
        const void* p0 = ci == 0 ? (const void*)(Vg + kvbase) : ci == 1 ? (const void*)w1 : ci == 2 ? (const void*)(w2 + (size_t)min(h, 27) * FS * DM) : (const void*)(wo + (size_t)h * DH * DM);
        const void* p1 = ci == 0 ? (const void*)(out + b * DM + 16 * h) : ci == 1 ? (const void*)(w1 + (size_t)(DM - 1) * FF) : ci == 2 ? (const void*)b1 : (const void*)b2;
        unsigned t0, t1;
        asm volatile("s_load_dword %0, %2, 0x0\n\ts_load_dword %1, %3, 0x0\n\ts_waitcnt lgkmcnt(0)" : "=&s"(t0), "=&s"(t1) : "s"(p0), "s"(p1) : "memory");
      }
    }
    AWAIT(C_Q, 1, 1);
    const u32x4 qp = *reinterpret_cast<const u32x4*>(&lds[L_Q + 4 * c]);
    float* sw = &lds[L_S + ci * 1024 + r * 4 + (c & 3)];
    const bool b0 = (c & 1) != 0, b1s = (c & 2) != 0;
    float m = -INFINITY;
#define KSTEP(n_, R_) do { \
      float sc[4]; \
      _Pragma("unroll") for (int t = 0; t < 4; ++t) \
        sc[t] = dot2(R_[t][3], qp[3], dot2(R_[t][2], qp[2], dot2(R_[t][1], qp[1], dot2(R_[t][0], qp[0], 0.f)))); \
        \
      const float e0 = (b0 ? sc[1] : sc[0]) + dpp<XOR1>(b0 ? sc[0] : sc[1]); \
      const float e1 = (b0 ? sc[3] : sc[2]) + dpp<XOR1>(b0 ? sc[2] : sc[3]); \
      float f = (b1s ? e1 : e0) + dpp<XOR2>(b1s ? e0 : e1); \
      f += dpp<ROR8>(f); f += dpp<ROR4>(f); \
      f = rnd_bf16(rnd_bf16(f) * SCALE);        \
      m = fmaxf(m, f); \
      if (c < 4) sw[(n_) * 16] = f; \
    } while (0)
    unsigned n = PRE;
    const unsigned* pfp = ci >= 2 ? reinterpret_cast<const unsigned*>(wo + (size_t)(h * DH + 16 * b + 8 * (ci - 2)) * DM) + 32 * lane
                        : ci == 1 ? reinterpret_cast<const unsigned*>(w2 + (size_t)(h * FS + 8 * b) * DM) + 32 * lane
                                  : reinterpret_cast<const unsigned*>(w1 + (size_t)(64 * b + lane) * FF + h * FS);
    const unsigned pfn = (ci >= 2 || h < FF / FS) ? PFN + PFD * ci : 0u;
    unsigned pfv = 0;
    u32x4 T[4];
    unsigned stop = NSLOT / 2;
    asm volatile("" : "+s"(stop));
#pragma nounroll
    for (;;) {
#pragma nounroll
      for (; n < stop; ++n) {
        TAKE(n, T);
        if (n == pfn) pfv = *pfp;
        KSTEP(n, T);
      }
      asm volatile("" :: "v"(pfv));
      if (stop == NSLOT) break;
      stop = NSLOT;
#pragma unroll
      for (int i = 0; i < PRE; ++i) KSTEP(i, R[i]);
    }
    m = xrow_max(fmaxf(fmaxf(m, dpp<XOR1>(m)), fmaxf(dpp<XOR2>(m), dpp<0x1B>(m))));
    if (lane == 0) ctl[C_M + ci] = __float_as_uint(m);
    ARRIVE(C_K);
    AWAIT(C_K, NC, 1);
    const float M2 = ROW_MAX();
    f32x2 acc[4] = {{0, 0}, {0, 0}, {0, 0}, {0, 0}};
    const float* pr = &lds[L_S + ci * 1024 + r * 4 - 16 * NSLOT];
#pragma nounroll
    for (; n < NRING; ++n) {
      f32x4 p = *reinterpret_cast<const f32x4*>(pr + 16 * n);
#pragma unroll
      for (int t = 0; t < 4; ++t) p[t] = __builtin_amdgcn_exp2f((p[t] - M2) * LOG2E);
      asm volatile("" : "+v"(p) :: "memory");
      TAKE(n, T);
#pragma unroll
      for (int t = 0; t < 4; ++t) PV_PIECE(T[t], p[t]);
    }
    PV_STORE(ci);
    __syncthreads();
  } else {
    const int hw = wave - 2 * NC, half = lane >> 5, pp = b & 1;
    const u16* xp = x + ((b & ~1) + half) * DM;
    const u32x4 xa0 = *reinterpret_cast<const u32x4*>(xp + 16 * (lane & 31)), xa1 = *reinterpret_cast<const u32x4*>(xp + 16 * (lane & 31) + 8);
    const u32x2 xs = *reinterpret_cast<const u32x2*>(xp + 128 * hw + 4 * (lane & 31));
    GAS unsigned* epoch = (GAS unsigned*)(ws + WS_EPOCH);
    unsigned tag2 = __hip_atomic_load(epoch + 64 * ((b & ~1) + half), __ATOMIC_RELAXED, __HIP_MEMORY_SCOPE_AGENT) + 1u;
    FENCE();
    u32x4 wv[32];
    const u16* pq = wq + (size_t)(128 * hw + 16 * (lane >> 3)) * (NH * DH) + h * DH + 64 * pp + 8 * (lane & 7);
#define LDQ(i) ldw(pq + (size_t)(((i) + 4 * (b >> 1)) & 15) * (NH * DH))
#pragma unroll
    for (int i = 0; i < QPACE; ++i) wv[i] = LDQ(i);
    FENCE();
    unsigned* xn = reinterpret_cast<unsigned*>(&lds[L_XN + hw * 128]);
    {
      float v[16];
#pragma unroll
      for (int j = 0; j < 4; ++j) { v[2 * j] = bf_lo(xa0[j]); v[2 * j + 1] = bf_hi(xa0[j]); v[8 + 2 * j] = bf_lo(xa1[j]); v[9 + 2 * j] = bf_hi(xa1[j]); }
      float s = 0.f;
#pragma unroll
      for (int j = 0; j < 16; j += 4) s += (v[j] + v[j + 1]) + (v[j + 2] + v[j + 3]);
      s = half_sum(s) * (1.0f / DM);
      float q2 = 0.f;
#pragma unroll
      for (int j = 0; j < 16; ++j) { const float d = v[j] - s; q2 = fmaf(d, d, q2); }
      q2 = half_sum(q2) * (1.0f / DM);
      const float mean = rnd_bf16(s);
      const float sdev = rnd_bf16(sqrtf(rnd_bf16(rnd_bf16(q2) + 1e-5f)));
      u32x2 o;
#pragma unroll
      for (int j = 0; j < 2; ++j) o[j] = pk_bf16(rnd_bf16(bf_lo(xs[j]) - mean) / sdev, rnd_bf16(bf_hi(xs[j]) - mean) / sdev);
      *reinterpret_cast<u32x2*>(xn + 2 * lane) = o;
      LGKM0();
    }
    {
      float qa[8] = {0, 0, 0, 0, 0, 0, 0, 0}, qb[8] = {0, 0, 0, 0, 0, 0, 0, 0};
      u32x2 xv[2][4];
#pragma unroll
      for (int k = 0; k < 4; ++k) {
        xv[0][k] = *reinterpret_cast<const u32x2*>(xn + 8 * (lane >> 3) + 2 * ((k + (b >> 1)) & 3));
        xv[1][k] = *reinterpret_cast<const u32x2*>(xn + 64 + 8 * (lane >> 3) + 2 * ((k + (b >> 1)) & 3));
      }
#pragma unroll
      for (int i = 0; i < 8; ++i) {
#pragma unroll
        for (int j = 0; j < 4; ++j) {
          const unsigned lo = __builtin_amdgcn_perm(wv[2 * i + 1][j], wv[2 * i][j], 0x05040100u), hi = __builtin_amdgcn_perm(wv[2 * i + 1][j], wv[2 * i][j], 0x07060302u);
          qa[2 * j] = dot2(lo, xv[0][i >> 1][i & 1], qa[2 * j]); qa[2 * j + 1] = dot2(hi, xv[0][i >> 1][i & 1], qa[2 * j + 1]);
          qb[2 * j] = dot2(lo, xv[1][i >> 1][i & 1], qb[2 * j]); qb[2 * j + 1] = dot2(hi, xv[1][i >> 1][i & 1], qb[2 * j + 1]);
        }
        asm volatile("" : "+v"(qb[0]), "+v"(qb[1]), "+v"(qb[2]), "+v"(qb[3]), "+v"(qb[4]), "+v"(qb[5]), "+v"(qb[6]), "+v"(qb[7]) :: "memory");
        FENCE();
        if (2 * i + QPACE < 16) { wv[2 * i + QPACE] = LDQ(2 * i + QPACE); wv[2 * i + QPACE + 1] = LDQ(2 * i + QPACE + 1); }
        FENCE();
      }
      const bool up = (c & 8) != 0;
#pragma unroll
      for (int j = 0; j < 8; ++j) qa[j] = (up ? qb[j] : qa[j]) + dpp<ROR8>(up ? qa[j] : qb[j]);
      float z0, z1;
      xrow_reduce8(qa, z0, z1);
      lds[L_QP + hw * 128 + 8 * c + r] = z0;
      lds[L_QP + hw * 128 + 8 * c + 4 + r] = z1;
      ARRIVE(C_QP);
    }
    const unsigned tagv = __builtin_amdgcn_readlane(tag2, 32 * pp);
    if (hw == 0) {
      AWAIT(C_QP, 4, 1);
      const float* qs = &lds[L_QP + 2 * lane];
      const f32x2 q2 = (*reinterpret_cast<const f32x2*>(qs) + *reinterpret_cast<const f32x2*>(qs + 128)) +
                       (*reinterpret_cast<const f32x2*>(qs + 256) + *reinterpret_cast<const f32x2*>(qs + 384));
      unsigned qv = pk_bf16(q2[0], q2[1]);
      auto rq = __builtin_amdgcn_make_buffer_rsrc((void*)(ws + WS_QX), 0, NB * NH * 256, 0x00020000);
      const bool other = half != pp;
      __builtin_amdgcn_raw_buffer_store_b64(u32x2{qv, tag2}, rq, other ? ((b ^ 1) * NH + h) * 256 + (lane & 31) * 8 : OOB, 0, 16);
      __builtin_amdgcn_s_sleep(QDELAY);
#pragma nounroll
      for (unsigned spins = 0;; ++spins) {
        asm volatile("" ::: "memory");
        const u32x2 g = __builtin_amdgcn_raw_buffer_load_b64(rq, other ? (b * NH + h) * 256 + (lane & 31) * 8 : OOB, 0, 16);
        if (__all(!other || g[1] == tagv)) { qv = other ? g[0] : qv; break; }
        if (spins > XSPIN_CAP) { qv = 0x7fc07fc0u; break; }
        __builtin_amdgcn_s_sleep(QSLP);
      }
      reinterpret_cast<unsigned*>(lds)[L_Q + lane] = qv;
      ARRIVE(C_Q);
    }
    FENCE();
#define HWAIT() vmwait<HPACE - 1>()
    AWAIT(C_DONE, h < FF / FS ? PFN + 3 : PFN + 3 * PFD + 3, 8);
    float y[8] = {0, 0, 0, 0, 0, 0, 0, 0};
    if (h < FF / FS) {
      {
        const u16* p1 = w1 + (size_t)(128 * hw + 16 * (lane >> 3)) * FF + h * FS + 8 * (lane & 7);
#pragma unroll
        for (int i = 0; i < 16; ++i) { HWAIT(); wv[i] = ldw(p1 + (size_t)((i + 2 * b) & 15) * FF); FENCE(); }
        float ha[8] = {0, 0, 0, 0, 0, 0, 0, 0};
#pragma unroll
        for (int i = 0; i < 8; ++i) gemv_pair(wv[2 * i], wv[2 * i + 1], xn[64 * pp + 8 * (lane >> 3) + ((i + b) & 7)], ha);
#pragma unroll
        for (int j = 0; j < 8; ++j) ha[j] += dpp<ROR8>(ha[j]);
        float z0, z1;
        xrow_reduce8(ha, z0, z1);
        if (c < 8) { lds[L_H + hw * 64 + 8 * c + r] = z0; lds[L_H + hw * 64 + 8 * c + 4 + r] = z1; }
        ARRIVE(C_H);
      }
      FENCE();
      {
        const u16* p2 = w2 + (size_t)(h * FS + r) * DM + 128 * hw + 8 * c;
#pragma unroll
        for (int i = 0; i < 16; ++i) { HWAIT(); wv[i] = ldw(p2 + (size_t)((i + 2 * b) & 15) * (4 * DM)); FENCE(); }
        const int f = 8 * ((c + b) & 7) + 4 * (c >> 3) + r;
        const u16 b1v = b1[h * FS + f];
        AWAIT(C_H, 4, 2);
        float hs = (lds[L_H + f] + lds[L_H + 64 + f]) + (lds[L_H + 128 + f] + lds[L_H + 192 + f]);
        hs = rnd_bf16(rnd_bf16(hs) + bf_lo(b1v));
        const float u = 0.7978845608028654f * fmaf(0.044715f * hs * hs, hs, hs);
        const float g = hs / (1.0f + __builtin_amdgcn_exp2f(-2.0f * LOG2E * u));
        gemv_bcast2<8>(wv, pk_bf16(g, dpp<ROR8>(g)), y);
      }
    }
    asm volatile("" : "+v"(y[0]), "+v"(y[1]), "+v"(y[2]), "+v"(y[3]), "+v"(y[4]), "+v"(y[5]), "+v"(y[6]), "+v"(y[7]));
    FENCE();
    unsigned wop[16][8];
    {
      const u16* po = wo + (size_t)(h * DH + r) * DM + 128 * hw + 8 * c;
#pragma unroll
      for (int i = 0; i < 32; ++i) { HWAIT(); wv[i] = ldw(po + (size_t)((i + 4 * b) & 31) * (4 * DM)); FENCE(); }
#pragma unroll
      for (int i = 0; i < 16; ++i) {
#pragma unroll
        for (int j = 0; j < 4; ++j) {
          wop[i][2 * j] = __builtin_amdgcn_perm(wv[2 * i + 1][j], wv[2 * i][j], 0x05040100u);
          wop[i][2 * j + 1] = __builtin_amdgcn_perm(wv[2 * i + 1][j], wv[2 * i][j], 0x07060302u);
        }
        asm volatile("" : "+v"(wop[i][0]), "+v"(wop[i][1]), "+v"(wop[i][2]), "+v"(wop[i][3]), "+v"(wop[i][4]), "+v"(wop[i][5]), "+v"(wop[i][6]), "+v"(wop[i][7]));
        FENCE();
      }
    }
    const int ocol = 16 * h + 8 * ((c >> 2) & 1) + (c & 3) + 4 * (r & 1);
    float base = bf_lo(x[b * DM + ocol]) + bf_lo(b2[ocol]);
    float invL;
    {
      AWAIT(C_K, NC, 8);
      const float M2 = ROW_MAX();
      float l = 0.f;
#pragma nounroll
      for (int k = 0; k < SK / 256; ++k) {
        const f32x4 s4 = *reinterpret_cast<const f32x4*>(&lds[L_S + 256 * k + 4 * lane]);
        l += (__builtin_amdgcn_exp2f((s4[0] - M2) * LOG2E) + __builtin_amdgcn_exp2f((s4[1] - M2) * LOG2E)) +
             (__builtin_amdgcn_exp2f((s4[2] - M2) * LOG2E) + __builtin_amdgcn_exp2f((s4[3] - M2) * LOG2E));
      }
      invL = 1.0f / xrow_sum(row_sum16(l));
    }
    asm volatile("" : "+v"(base), "+v"(invL));
    FENCE();
    auto rs = __builtin_amdgcn_make_buffer_rsrc((void*)(ws + WS_GRAN + (size_t)b * NH * NH * 128), 0, NH * NH * 128, 0x00020000);
    __syncthreads();
    {
      const f32x2* rec = reinterpret_cast<const f32x2*>(&lds[L_REC + 2 * (16 * r + ((c + 2 * b) & 15))]);
      f32x2 o2 = (rec[0] + rec[64]) + (rec[128] + rec[192]);
      o2 += (rec[256] + rec[320]) + (rec[384] + rec[448]);
      o2 *= invL;
      gemv_bcast<16>(wop, pk_bf16(o2[0], o2[1]), y);
    }
    float z0, z1;
    xrow_reduce8(y, z0, z1);
    __builtin_amdgcn_raw_buffer_store_b128(u32x4{__float_as_uint(z0), tagv, __float_as_uint(z1), tagv}, rs,
                                           ((8 * hw + (c >> 1)) * NH + h) * 128 + (4 * (c & 1) + r) * 16, 0, 16);
    if (hw == 0) {
      float v0[4], v1[4];
      unsigned off[4];
#pragma unroll
      for (int k = 0; k < 4; ++k) { v0[k] = v1[k] = 0.f; off[k] = h * NH * 128 + lane * 16 + 1024 * k; }
      __builtin_amdgcn_s_sleep(PDELAY);
#pragma nounroll
      for (unsigned spins = 0;; ++spins) {
        asm volatile("" ::: "memory");
        u32x4 gv[4];
#pragma unroll
        for (int k = 0; k < 4; ++k) gv[k] = __builtin_amdgcn_raw_buffer_load_b128(rs, off[k], 0, 16);
        bool ok = true;
#pragma unroll
        for (int k = 0; k < 4; ++k) {
          const bool hit = off[k] < OOB && gv[k][1] == tagv && gv[k][3] == tagv;
          v0[k] = hit ? __uint_as_float(gv[k][0]) : v0[k];
          v1[k] = hit ? __uint_as_float(gv[k][2]) : v1[k];
          off[k] = hit ? OOB : off[k];
          ok &= off[k] >= OOB;
        }
        if (__all(ok)) break;
        if (spins > XSPIN_CAP) { v0[0] = __builtin_nanf(""); break; }
        __builtin_amdgcn_s_sleep(1);
      }
      float t0 = (v0[0] + v0[1]) + (v0[2] + v0[3]), t1 = (v1[0] + v1[1]) + (v1[2] + v1[3]);
      t0 += dpp<ROR8>(t0); t1 += dpp<ROR8>(t1);
      auto sw16 = __builtin_amdgcn_permlane16_swap(__float_as_uint(t0), __float_as_uint(t1), false, false);
      float tt = __uint_as_float(sw16[0]) + __uint_as_float(sw16[1]);
      auto sw32 = __builtin_amdgcn_permlane32_swap(__float_as_uint(tt), __float_as_uint(tt), false, false);
      tt = __uint_as_float(sw32[0]) + __uint_as_float(sw32[1]);
      auto ro = __builtin_amdgcn_make_buffer_rsrc((void*)(out + b * DM), 0, DM * 2, 0x00020000);
      __builtin_amdgcn_raw_buffer_store_b16((u16)(pk_bf16(base + tt, 0.f) & 0xffffu), ro, (lane < 32 && c < 8) ? ocol * 2 : OOB, 0, 0);
      if (h == 0 && lane == 0) __hip_atomic_store(epoch + 64 * b, tagv, __ATOMIC_RELAXED, __HIP_MEMORY_SCOPE_AGENT);
    }
  }
}

extern "C" void kernel_launch(void* const* d_in, const int* in_sizes, int n_in, void* d_out, int out_size, void* d_ws, size_t ws_size,
                              hipStream_t stream) {
  const u16* x = (const u16*)d_in[0];
  const u16* K = (const u16*)d_in[1];
  const u16* V = (const u16*)d_in[2];
  const u16* wq = (const u16*)d_in[3];
  const u16* wo = (const u16*)d_in[4];
  const u16* w1 = (const u16*)d_in[6];
  const u16* b1 = (const u16*)d_in[7];
  const u16* w2 = (const u16*)d_in[8];
  const u16* b2 = (const u16*)d_in[9];
  fused_decode<<<NB * NH, NTHR, 0, stream>>>(x, K, V, wq, (unsigned char*)d_ws, wo, w1, w2, b1, b2, (u16*)d_out);
}
```
